```python
import torch

def _softmax(x, axis=-1):
    return torch.softmax(x, dim=axis)
def _gelu(x):
    return torch.nn.functional.gelu(x, approximate="tanh")
def _silu(x):
    return torch.nn.functional.silu(x)
def _sigmoid(x):
    return torch.sigmoid(x)
def _layernorm(x, axis=-1, eps=1e-5):
    m = x.mean(dim=axis, keepdim=True)
    v = x.var(dim=axis, keepdim=True, unbiased=False)
    return (x - m) / torch.sqrt(v + eps)
def _scatter_add(tgt, idx, src, axis=0):
    return tgt.clone().index_add_(axis, idx, src)
def _segment_sum(src, seg, n):
    out = torch.zeros((n,) + src.shape[1:], dtype=src.dtype)
    return out.index_add_(0, seg, src)
def _pad(x, pw):
    flat = tuple(v for pair in reversed(pw) for v in pair)
    return torch.nn.functional.pad(x, flat)

kernel_name = 'concat_heads__gqa_block_cached__gqa_block_cached__decode'

def setup_inputs():

    _w = (lambda a: (lambda t: t.to(torch.bfloat16) if t.is_floating_point() else t)(torch.as_tensor(a)))
    def _A_setup():
        B, Sq, D = 2, 1, 640
        Hq, Hkv, Sk, Dh = 32, 32, 4096, 128
        assert Hq % Hkv == 0, (Hq, Hkv)
        return {
            "x": _w((lambda *s: torch.randn(*s, dtype=torch.bfloat16) * 0.8)(B, Sq, D)),
            "k_cache": _w((lambda *s: torch.randn(*s, dtype=torch.bfloat16) * 0.8)(B, Hkv, Sk, Dh)),
            "v_cache": _w((lambda *s: torch.randn(*s, dtype=torch.bfloat16) * 0.05)(B, Hkv, Sk, Dh)),
            "wq": _w((lambda *s: torch.randn(*s, dtype=torch.bfloat16) * 0.8)(D, Hq * Dh) * (D ** -0.5)),
            "wo": _w((lambda *s: torch.randn(*s, dtype=torch.bfloat16) * 0.05)(Hq * Dh, D) * ((Hq * Dh) ** -0.5)),
        }
    _A_in = _A_setup()

    def _B_setup():
        B, Sq, D = 2, 1, 640
        Hq, Hkv, Sk, Dh = 32, 32, 4096, 128
        assert Hq % Hkv == 0, (Hq, Hkv)
        return {
            "x": _w((lambda *s: torch.randn(*s, dtype=torch.bfloat16) * 0.8)(B, Sq, D)),
            "k_cache": _w((lambda *s: torch.randn(*s, dtype=torch.bfloat16) * 0.8)(B, Hkv, Sk, Dh)),
            "v_cache": _w((lambda *s: torch.randn(*s, dtype=torch.bfloat16) * 0.05)(B, Hkv, Sk, Dh)),
            "wq": _w((lambda *s: torch.randn(*s, dtype=torch.bfloat16) * 0.8)(D, Hq * Dh) * (D ** -0.5)),
            "wo": _w((lambda *s: torch.randn(*s, dtype=torch.bfloat16) * 0.05)(Hq * Dh, D) * ((Hq * Dh) ** -0.5)),
        }
    _B_in = _B_setup()

    return {
        "A_x": _A_in["x"],
        "A_k_cache": _A_in["k_cache"],
        "A_v_cache": _A_in["v_cache"],
        "A_wq": _A_in["wq"],
        "A_wo": _A_in["wo"],
        "B_x": _B_in["x"],
        "B_k_cache": _B_in["k_cache"],
        "B_v_cache": _B_in["v_cache"],
        "B_wq": _B_in["wq"],
        "B_wo": _B_in["wo"],
    }

def reference(A_x, A_k_cache, A_v_cache, A_wq, A_wo, B_x, B_k_cache, B_v_cache, B_wq, B_wo):
    def _A_ref(x, k_cache, v_cache, wq, wo):
        B, Sq, D = x.shape
        Hq, Dh = 32, 128
        Hkv = k_cache.shape[1]
        rep = Hq // Hkv
        q = (lambda x, *d: x.permute(*d))((x @ wq).reshape(B, Sq, Hq, Dh), 0, 2, 1, 3)
        k = (lambda x, n, axis: torch.repeat_interleave(x, n, dim=axis))(k_cache, rep, axis=1)
        v = (lambda x, n, axis: torch.repeat_interleave(x, n, dim=axis))(v_cache, rep, axis=1)
        scores = torch.einsum("bhqd,bhkd->bhqk", q, k) * (Dh ** -0.5)
        p = _softmax(scores, axis=-1)
        o = torch.einsum("bhqk,bhkd->bhqd", p, v)
        return (lambda x, *d: x.permute(*d))(o, 0, 2, 1, 3).reshape(B, Sq, Hq * Dh) @ wo

    def _B_ref(x, k_cache, v_cache, wq, wo):
        B, Sq, D = x.shape
        Hq, Dh = 32, 128
        Hkv = k_cache.shape[1]
        rep = Hq // Hkv
        q = (lambda x, *d: x.permute(*d))((x @ wq).reshape(B, Sq, Hq, Dh), 0, 2, 1, 3)
        k = (lambda x, n, axis: torch.repeat_interleave(x, n, dim=axis))(k_cache, rep, axis=1)
        v = (lambda x, n, axis: torch.repeat_interleave(x, n, dim=axis))(v_cache, rep, axis=1)
        scores = torch.einsum("bhqd,bhkd->bhqk", q, k) * (Dh ** -0.5)
        p = _softmax(scores, axis=-1)
        o = torch.einsum("bhqk,bhkd->bhqd", p, v)
        return (lambda x, *d: x.permute(*d))(o, 0, 2, 1, 3).reshape(B, Sq, Hq * Dh) @ wo

    a = _A_ref(A_x, A_k_cache, A_v_cache, A_wq, A_wo)
    b = _B_ref(B_x, B_k_cache, B_v_cache, B_wq, B_wo)
    return torch.concatenate([a, b], axis=-1)
```

```cpp
#include <hip/hip_runtime.h>
#include <hip/hip_bf16.h>
#include <stdint.h>

namespace {

constexpr int DM = 640, NH = 32, SK = 4096, DH = 128, NB = 2;
constexpr int NSPLIT = 2;
constexpr int NWAVE = 8, NTHR = NWAVE * 64;
constexpr int KEYS_WG = SK / NSPLIT;
constexpr int KEYS_WAVE = KEYS_WG / NWAVE;
constexpr int NTILE = KEYS_WAVE / 4;
constexpr int WIN = 16;
constexpr int NIT = NTILE / WIN;
constexpr int NWG = 2 * NB * NH * NSPLIT;
constexpr int NPART = NH * NSPLIT;

typedef unsigned u32x4 __attribute__((ext_vector_type(4)));
typedef unsigned u32x2 __attribute__((ext_vector_type(2)));
typedef float f32x4 __attribute__((ext_vector_type(4)));
typedef float f32x2 __attribute__((ext_vector_type(2)));
typedef __bf16 bf16x2 __attribute__((ext_vector_type(2)));

__device__ __forceinline__ float dot2(unsigned a, unsigned b, float acc) {
  return __builtin_amdgcn_fdot2_f32_bf16(__builtin_bit_cast(bf16x2, a), __builtin_bit_cast(bf16x2, b), acc, false);
}
__device__ __forceinline__ unsigned cvtpk(float lo, float hi) {
  bf16x2 r;
  r[0] = (__bf16)lo;
  r[1] = (__bf16)hi;
  return __builtin_bit_cast(unsigned, r);
}
template <int CTRL> __device__ __forceinline__ float dpp(float x) {
  return __builtin_bit_cast(float, __builtin_amdgcn_mov_dpp(__builtin_bit_cast(int, x), CTRL, 0xf, 0xf, true));
}
constexpr int XOR1 = 0xB1, XOR2 = 0x4E, XOR7 = 0x141, XOR8 = 0x128, ROR4 = 0x124, ROR2 = 0x122, ROR1 = 0x121;
__device__ __forceinline__ unsigned perm_lo(unsigned a, unsigned b) { return __builtin_amdgcn_perm(b, a, 0x05040100u); }
__device__ __forceinline__ unsigned perm_hi(unsigned a, unsigned b) { return __builtin_amdgcn_perm(b, a, 0x07060302u); }

__device__ __forceinline__ float xrow_max(float x) {
  auto s = __builtin_amdgcn_permlane16_swap(__float_as_uint(x), __float_as_uint(x), false, false);
  x = fmaxf(__uint_as_float(s[0]), __uint_as_float(s[1]));
  auto t = __builtin_amdgcn_permlane32_swap(__float_as_uint(x), __float_as_uint(x), false, false);
  return fmaxf(__uint_as_float(t[0]), __uint_as_float(t[1]));
}
__device__ __forceinline__ float xrow_sum(float x) {
  auto s = __builtin_amdgcn_permlane16_swap(__float_as_uint(x), __float_as_uint(x), false, false);
  x = __uint_as_float(s[0]) + __uint_as_float(s[1]);
  auto t = __builtin_amdgcn_permlane32_swap(__float_as_uint(x), __float_as_uint(x), false, false);
  return __uint_as_float(t[0]) + __uint_as_float(t[1]);
}
__device__ __forceinline__ float wave_max(float x) {
  x = fmaxf(x, dpp<XOR1>(x)); x = fmaxf(x, dpp<XOR2>(x)); x = fmaxf(x, dpp<XOR7>(x)); x = fmaxf(x, dpp<XOR8>(x));
  return xrow_max(x);
}
__device__ __forceinline__ float wave_sum(float x) {
  x += dpp<XOR1>(x); x += dpp<XOR2>(x); x += dpp<XOR7>(x); x += dpp<XOR8>(x);
  return xrow_sum(x);
}
__device__ __forceinline__ u32x4 ldnt(const char* p) { return __builtin_nontemporal_load(reinterpret_cast<const u32x4*>(p)); }
__device__ __forceinline__ u32x4 ld(const char* p) { return *reinterpret_cast<const u32x4*>(p); }

struct Args {
  const char* x[2];
  const char* k[2];
  const char* v[2];
  const char* wq[2];
  const char* wo[2];
  float* part;
  float* ml;
  __hip_bfloat16* out;
};

template <int T> struct Bc { static constexpr int v = 0x150 + T; };

__global__ void __launch_bounds__(NTHR, 2) attn_main(Args a) {
  __shared__ __attribute__((aligned(16))) float lds[8 * 640 + 64];
  const int tid = threadIdx.x, lane = tid & 63, w = __builtin_amdgcn_readfirstlane(tid >> 6);
  const int g = lane >> 4, c = lane & 15;
  const int bid = blockIdx.x, xcd = bid & 7, idx = bid >> 3;
  const int grp = xcd * 8 + (idx >> 2), mem = idx & 3;
  const int br = grp >> 5, h = grp & 31, b = mem >> 1, sp = mem & 1;

  const char* wqp = a.wq[br] + ((size_t)(80 * w + 20 * g) * (NH * DH) + h * DH + 8 * c) * 2;
  u32x4 wr[20];
#pragma unroll
  for (int j = 0; j < 20; ++j) wr[j] = ld(wqp + (size_t)j * (NH * DH * 2));
  u32x2 xr[5];
  {
    const char* xp = a.x[br] + (b * DM + 80 * w + 20 * g) * 2;
#pragma unroll
    for (int j = 0; j < 5; ++j) xr[j] = *reinterpret_cast<const u32x2*>(xp + 8 * j);
  }
  __builtin_amdgcn_sched_barrier(0);
  const size_t kvoff = (((size_t)(b * NH + h) * SK + sp * KEYS_WG + w * KEYS_WAVE) * DH) * 2 + lane * 16;
  const char* kp = a.k[br] + kvoff;
  const char* vp = a.v[br] + kvoff;
  u32x4 ring[WIN];
#pragma unroll
  for (int t = 0; t < WIN; ++t) ring[t] = ldnt(kp + t * 1024);
  __builtin_amdgcn_sched_barrier(0);

  float qa[8];
#pragma unroll
  for (int i = 0; i < 8; ++i) qa[i] = 0.f;
#pragma unroll
  for (int j = 0; j < 10; ++j) {
    const unsigned xx = xr[j >> 1][j & 1];
#pragma unroll
    for (int d = 0; d < 4; ++d) {
      qa[2 * d] = dot2(perm_lo(wr[2 * j][d], wr[2 * j + 1][d]), xx, qa[2 * d]);
      qa[2 * d + 1] = dot2(perm_hi(wr[2 * j][d], wr[2 * j + 1][d]), xx, qa[2 * d + 1]);
    }
  }
  __builtin_amdgcn_sched_barrier(0);
  const char* wop = a.wo[br] + ((size_t)(h * DH + 16 * w + 4 * g) * DM + 8 * c) * 2;
#pragma unroll
  for (int cb = 0; cb < 5; ++cb)
#pragma unroll
    for (int j = 0; j < 4; ++j) wr[cb * 4 + j] = ld(wop + j * (DM * 2) + cb * 256);
  __builtin_amdgcn_sched_barrier(0);

  *reinterpret_cast<f32x4*>(&lds[(w * 4 + g) * 128 + 8 * c]) = f32x4{qa[0], qa[1], qa[2], qa[3]};
  *reinterpret_cast<f32x4*>(&lds[(w * 4 + g) * 128 + 8 * c + 4]) = f32x4{qa[4], qa[5], qa[6], qa[7]};
  __syncthreads();
  if (tid < 64) {
    float s0 = 0.f, s1 = 0.f;
#pragma unroll
    for (int i = 0; i < 32; ++i) {
      const f32x2 t = *reinterpret_cast<const f32x2*>(&lds[i * 128 + 2 * tid]);
      s0 += t[0]; s1 += t[1];
    }
    reinterpret_cast<unsigned*>(lds + 4096)[tid] = cvtpk(s0, s1);
  }
  __syncthreads();
  const u32x4 q = *reinterpret_cast<const u32x4*>(reinterpret_cast<const unsigned*>(lds + 4096) + 4 * c);

  float sc[NIT];
#pragma unroll
  for (int it = 0; it < NIT; ++it) {
    const char* nx = (it + 1 < NIT) ? kp + (it + 1) * (WIN * 1024) : vp;
    float x[WIN];
#pragma unroll
    for (int t = 0; t < WIN; ++t) {
      const u32x4 cur = ring[t];
      ring[t] = ldnt(nx + t * 1024);
      float d = dot2(cur[0], q[0], 0.f);
      d = dot2(cur[1], q[1], d);
      d = dot2(cur[2], q[2], d);
      d = dot2(cur[3], q[3], d);
      x[t] = d;
      __builtin_amdgcn_sched_barrier(0);
    }
#define TR_STEP(HALF, CTRL, BIT)                                             \
  _Pragma("unroll") for (int hh = 0; hh < (HALF); ++hh) {                    \
    const float lo_ = x[hh], hi_ = x[hh + (HALF)];                           \
    const float t1_ = lo_ + dpp<CTRL>(lo_), t2_ = hi_ + dpp<CTRL>(hi_);      \
    x[hh] = (c & (BIT)) ? t2_ : t1_;                                         \
  }
    TR_STEP(8, XOR8, 8) TR_STEP(4, XOR7, 4) TR_STEP(2, XOR2, 2) TR_STEP(1, XOR1, 1)
#undef TR_STEP
    sc[it] = x[0];
    __builtin_amdgcn_sched_barrier(0);
  }
  constexpr float C2 = 0.088388347648318440f * 1.4426950408889634f;
  float mx = sc[0];
#pragma unroll
  for (int it = 1; it < NIT; ++it) mx = fmaxf(mx, sc[it]);
  mx = wave_max(mx);
  float* red = lds + 4096 + 64;
  if (lane == 0) red[w] = mx;
  __syncthreads();
  {
    const f32x4 m0 = *reinterpret_cast<const f32x4*>(red), m1 = *reinterpret_cast<const f32x4*>(red + 4);
    mx = fmaxf(fmaxf(fmaxf(m0[0], m0[1]), fmaxf(m0[2], m0[3])), fmaxf(fmaxf(m1[0], m1[1]), fmaxf(m1[2], m1[3])));
  }
  const float mneg = -mx * C2;
  float ls = 0.f;
#pragma unroll
  for (int it = 0; it < NIT; ++it) { sc[it] = __builtin_amdgcn_exp2f(fmaf(sc[it], C2, mneg)); ls += sc[it]; }
  ls = wave_sum(ls);
  if (lane == 0) red[8 + w] = ls;
#pragma unroll
  for (int cb = 0; cb < 5; ++cb)
#pragma unroll
    for (int jj = 0; jj < 2; ++jj)
#pragma unroll
      for (int d = 0; d < 4; ++d) {
        const unsigned r0 = wr[cb * 4 + 2 * jj][d], r1 = wr[cb * 4 + 2 * jj + 1][d];
        wr[cb * 4 + 2 * jj][d] = perm_lo(r0, r1);
        wr[cb * 4 + 2 * jj + 1][d] = perm_hi(r0, r1);
      }
  __builtin_amdgcn_sched_barrier(0);

  float acc[8];
#pragma unroll
  for (int i = 0; i < 8; ++i) acc[i] = 0.f;
#define VSTEP(T)                                                                        \
  {                                                                                     \
    const u32x4 cur = ring[T];                                                          \
    if (it + 1 < NIT) ring[T] = ldnt(vp + (it + 1) * (WIN * 1024) + (T) * 1024);        \
    const float p = dpp<0x150 + (T)>(sc[it]);                                           \
    _Pragma("unroll") for (int d = 0; d < 4; ++d) {                                     \
      acc[2 * d] = fmaf(p, __uint_as_float(cur[d] << 16), acc[2 * d]);                  \
      acc[2 * d + 1] = fmaf(p, __uint_as_float(cur[d] & 0xffff0000u), acc[2 * d + 1]);  \
    }                                                                                   \
    __builtin_amdgcn_sched_barrier(0);                                                  \
  }
#pragma unroll
  for (int it = 0; it < NIT; ++it) {
    VSTEP(0) VSTEP(1) VSTEP(2) VSTEP(3) VSTEP(4) VSTEP(5) VSTEP(6) VSTEP(7)
    VSTEP(8) VSTEP(9) VSTEP(10) VSTEP(11) VSTEP(12) VSTEP(13) VSTEP(14) VSTEP(15)
  }
#undef VSTEP
#pragma unroll
  for (int i = 0; i < 8; ++i) acc[i] = xrow_sum(acc[i]);
  if (g == 0) {
    *reinterpret_cast<f32x4*>(&lds[w * 128 + 8 * c]) = f32x4{acc[0], acc[1], acc[2], acc[3]};
    *reinterpret_cast<f32x4*>(&lds[w * 128 + 8 * c + 4]) = f32x4{acc[4], acc[5], acc[6], acc[7]};
  }
  __syncthreads();
  f32x4 o4 = {0.f, 0.f, 0.f, 0.f};
#pragma unroll
  for (int ww = 0; ww < NWAVE; ++ww) o4 += *reinterpret_cast<const f32x4*>(&lds[ww * 128 + 16 * w + 4 * g]);
  float lsum;
  {
    const f32x4 l0 = *reinterpret_cast<const f32x4*>(red + 8), l1 = *reinterpret_cast<const f32x4*>(red + 12);
    lsum = ((l0[0] + l0[1]) + (l0[2] + l0[3])) + ((l1[0] + l1[1]) + (l1[2] + l1[3]));
  }
  const float inv = 1.f / lsum;
  const unsigned o01 = cvtpk(o4[0] * inv, o4[1] * inv), o23 = cvtpk(o4[2] * inv, o4[3] * inv);
  float y[40];
#pragma unroll
  for (int cb = 0; cb < 5; ++cb)
#pragma unroll
    for (int d = 0; d < 4; ++d) {
      y[cb * 8 + 2 * d] = dot2(wr[cb * 4 + 2][d], o23, dot2(wr[cb * 4][d], o01, 0.f));
      y[cb * 8 + 2 * d + 1] = dot2(wr[cb * 4 + 3][d], o23, dot2(wr[cb * 4 + 1][d], o01, 0.f));
    }
#pragma unroll
  for (int i = 0; i < 40; ++i) y[i] = xrow_sum(y[i]);
  __syncthreads();
  if (g == 0) {
#pragma unroll
    for (int cb = 0; cb < 5; ++cb) {
      *reinterpret_cast<f32x4*>(&lds[w * 640 + cb * 128 + 8 * c]) = f32x4{y[cb * 8], y[cb * 8 + 1], y[cb * 8 + 2], y[cb * 8 + 3]};
      *reinterpret_cast<f32x4*>(&lds[w * 640 + cb * 128 + 8 * c + 4]) = f32x4{y[cb * 8 + 4], y[cb * 8 + 5], y[cb * 8 + 6], y[cb * 8 + 7]};
    }
  }
  __syncthreads();
  const int row = br * NB + b, pi = h * NSPLIT + sp;
  if (tid < 160) {
    f32x4 s = {0.f, 0.f, 0.f, 0.f};
#pragma unroll
    for (int ww = 0; ww < NWAVE; ++ww) s += *reinterpret_cast<const f32x4*>(&lds[ww * 640 + 4 * tid]);
    *reinterpret_cast<f32x4*>(a.part + ((size_t)row * NPART + pi) * DM + 4 * tid) = s;
  }
  if (tid == 0) *reinterpret_cast<f32x2*>(a.ml + (row * NPART + pi) * 2) = f32x2{mx * C2, lsum};
}

__global__ void __launch_bounds__(256) attn_reduce(Args a) {
  __shared__ __attribute__((aligned(16))) float red[16 * 64];
  const int tid = threadIdx.x, cq = tid & 15, pg = tid >> 4;
  const int row = blockIdx.y, col0 = blockIdx.x * 64;
  f32x4 v[4];
#pragma unroll
  for (int i = 0; i < 4; ++i) v[i] = *reinterpret_cast<const f32x4*>(a.part + ((size_t)row * NPART + pg * 4 + i) * DM + col0 + 4 * cq);
  f32x4 s = {0.f, 0.f, 0.f, 0.f};
#pragma unroll
  for (int hh = 0; hh < 2; ++hh) {
    const f32x4 ml = *reinterpret_cast<const f32x4*>(a.ml + (row * NPART + pg * 4 + 2 * hh) * 2);
    const float M = fmaxf(ml[0], ml[2]);
    const float w0 = ml[1] * __builtin_amdgcn_exp2f(ml[0] - M), w1 = ml[3] * __builtin_amdgcn_exp2f(ml[2] - M);
    const float inv = 1.f / (w0 + w1);
    s += v[2 * hh] * (w0 * inv) + v[2 * hh + 1] * (w1 * inv);
  }
  *reinterpret_cast<f32x4*>(&red[pg * 64 + 4 * cq]) = s;
  __syncthreads();
  if (tid < 64) {
    float t = 0.f;
#pragma unroll
    for (int i = 0; i < 16; ++i) t += red[i * 64 + tid];
    const int br = row >> 1, b = row & 1;
    a.out[b * (2 * DM) + br * DM + col0 + tid] = __float2bfloat16(t);
  }
}

}

extern "C" void kernel_launch(void* const* d_in, const int* in_sizes, int n_in, void* d_out, int out_size, void* d_ws,
                              size_t ws_size, hipStream_t stream) {
  Args a;
  for (int br = 0; br < 2; ++br) {
    a.x[br] = (const char*)d_in[5 * br + 0];
    a.k[br] = (const char*)d_in[5 * br + 1];
    a.v[br] = (const char*)d_in[5 * br + 2];
    a.wq[br] = (const char*)d_in[5 * br + 3];
    a.wo[br] = (const char*)d_in[5 * br + 4];
  }
  a.part = (float*)d_ws;
  a.ml = (float*)((char*)d_ws + (size_t)4 * NPART * DM * 4);
  a.out = (__hip_bfloat16*)d_out;
  hipLaunchKernelGGL(attn_main, dim3(NWG), dim3(NTHR), 0, stream, a);
  hipLaunchKernelGGL(attn_reduce, dim3(10, 4), dim3(256), 0, stream, a);
}
```

```cpp
#include <hip/hip_runtime.h>
#include <hip/hip_bf16.h>
#include <stdint.h>

#define WIN 4
#define AUX_W 2
#define ROTMUL 1
#define WO_LATE 0
#define STAMPS 0

namespace {

constexpr int DM = 640, NH = 32, SK = 4096, DH = 128, NB = 2;
constexpr int NSPLIT = 2;
constexpr int NWAVE = 8, NTHR = NWAVE * 64;
constexpr int KEYS_WG = SK / NSPLIT;
constexpr int NSTEP = KEYS_WG / (4 * NWAVE);
constexpr int STEP_B = NWAVE * 1024;
constexpr int NBODY = NSTEP / 4;
constexpr int NWG = 2 * NB * NH * NSPLIT;
constexpr int NPART = NH * NSPLIT;
constexpr int AUX_NT = 2;

typedef unsigned u32x4 __attribute__((ext_vector_type(4)));
typedef unsigned u32x2 __attribute__((ext_vector_type(2)));
typedef float f32x4 __attribute__((ext_vector_type(4)));
typedef float f32x2 __attribute__((ext_vector_type(2)));
typedef __bf16 bf16x2 __attribute__((ext_vector_type(2)));

__device__ __forceinline__ float dot2(unsigned a, unsigned b, float acc) {
  return __builtin_amdgcn_fdot2_f32_bf16(__builtin_bit_cast(bf16x2, a), __builtin_bit_cast(bf16x2, b), acc, false);
}
__device__ __forceinline__ unsigned cvtpk(float lo, float hi) {
  bf16x2 r;
  r[0] = (__bf16)lo;
  r[1] = (__bf16)hi;
  return __builtin_bit_cast(unsigned, r);
}
template <int CTRL> __device__ __forceinline__ float dpp(float x) {
  return __builtin_bit_cast(float, __builtin_amdgcn_mov_dpp(__builtin_bit_cast(int, x), CTRL, 0xf, 0xf, true));
}
constexpr int XOR1 = 0xB1, XOR2 = 0x4E, XOR7 = 0x141, XOR8 = 0x128;
__device__ __forceinline__ unsigned perm_lo(unsigned a, unsigned b) { return __builtin_amdgcn_perm(b, a, 0x05040100u); }
__device__ __forceinline__ unsigned perm_hi(unsigned a, unsigned b) { return __builtin_amdgcn_perm(b, a, 0x07060302u); }

__device__ __forceinline__ float xrow_max(float x) {
  auto s = __builtin_amdgcn_permlane16_swap(__float_as_uint(x), __float_as_uint(x), false, false);
  x = fmaxf(__uint_as_float(s[0]), __uint_as_float(s[1]));
  auto t = __builtin_amdgcn_permlane32_swap(__float_as_uint(x), __float_as_uint(x), false, false);
  return fmaxf(__uint_as_float(t[0]), __uint_as_float(t[1]));
}
__device__ __forceinline__ float xrow_sum(float x) {
  auto s = __builtin_amdgcn_permlane16_swap(__float_as_uint(x), __float_as_uint(x), false, false);
  x = __uint_as_float(s[0]) + __uint_as_float(s[1]);
  auto t = __builtin_amdgcn_permlane32_swap(__float_as_uint(x), __float_as_uint(x), false, false);
  return __uint_as_float(t[0]) + __uint_as_float(t[1]);
}
__device__ __forceinline__ float fold32(float a, float b) {
  auto t = __builtin_amdgcn_permlane32_swap(__float_as_uint(a), __float_as_uint(b), false, false);
  return __uint_as_float(t[0]) + __uint_as_float(t[1]);
}
__device__ __forceinline__ float fold16(float a, float b) {
  auto t = __builtin_amdgcn_permlane16_swap(__float_as_uint(a), __float_as_uint(b), false, false);
  return __uint_as_float(t[0]) + __uint_as_float(t[1]);
}

struct Args {
  const char* x[2];
  const char* k[2];
  const char* v[2];
  const char* wq[2];
  const char* wo[2];
  float* part;
  float* ml;
  __hip_bfloat16* out;
  unsigned long long* ts;
};

#define RSRC(p, n) __builtin_amdgcn_make_buffer_rsrc((void*)(p), 0, (n), 0x00020000)
#define BLD(r, vo, so, aux) __builtin_amdgcn_raw_buffer_load_b128((r), (vo), (so), (aux))
#define STAMP(i)

constexpr int L_SC = 0;
constexpr int L_Q = 2048;
constexpr int L_RED = L_Q + 8 * 640;
constexpr int L_TOT = L_RED + 64 + 16;

__global__ void __launch_bounds__(NTHR, 2) attn_main(Args a) {
  __shared__ __attribute__((aligned(16))) float lds[L_TOT];
  const int tid = threadIdx.x, lane = tid & 63, w = __builtin_amdgcn_readfirstlane(tid >> 6);
  const int g = lane >> 4, c = lane & 15;
  STAMP(0);
  const int bid = blockIdx.x, xcd = bid & 7, idx = bid >> 3;
  const int grp = xcd * 8 + (idx >> 2), mem = idx & 3;
  const int br = grp >> 5, h = grp & 31, b = mem >> 1, sp = mem & 1;
  const int rot = ((grp * 4 + mem) * ROTMUL) & (NSTEP - 1);

  u32x2 xr[5];
  {
    const char* xp = a.x[br] + (b * DM + 80 * w + 20 * g) * 2;
#pragma unroll
    for (int j = 0; j < 5; ++j) xr[j] = *reinterpret_cast<const u32x2*>(xp + 8 * j);
  }
  const auto wqr = RSRC(a.wq[br] + h * DH * 2, DM * NH * DH * 2 - h * DH * 2);
  const int wq_vo = (80 * w + 20 * g) * (NH * DH * 2) + 16 * c;
  u32x4 wq[20];
#pragma unroll
  for (int j = 0; j < 20; ++j) wq[j] = BLD(wqr, wq_vo, j * (NH * DH * 2), AUX_W);
  __builtin_amdgcn_sched_barrier(0);
  const auto wor = RSRC(a.wo[br] + (size_t)h * DH * DM * 2, DH * DM * 2);
  const int wo_vo = (16 * w + 4 * g) * (DM * 2) + 16 * c;
  u32x4 wo[20];
#pragma unroll
  for (int cb = 0; cb < 5; ++cb)
#pragma unroll
    for (int j = 0; j < 4; ++j) wo[cb * 4 + j] = BLD(wor, wo_vo, j * (DM * 2) + cb * 256, AUX_W);
  __builtin_amdgcn_sched_barrier(0);
  const size_t kvoff = ((size_t)(b * NH + h) * SK + sp * KEYS_WG) * (DH * 2);
  const auto kr = RSRC(a.k[br] + kvoff, KEYS_WG * DH * 2);
  const auto vr = RSRC(a.v[br] + kvoff, KEYS_WG * DH * 2);
  const int kv_vo = w * 1024 + lane * 16;
#define SOFF(s) ((((s) + rot) & (NSTEP - 1)) * STEP_B)
  u32x4 ring[4];
#pragma unroll
  for (int t = 0; t < 4; ++t) ring[t] = BLD(kr, kv_vo, SOFF(t), AUX_NT);
  __builtin_amdgcn_sched_barrier(0);

  float qa[8];
#pragma unroll
  for (int i = 0; i < 8; ++i) qa[i] = 0.f;
#pragma unroll
  for (int j = 0; j < 10; ++j) {
    const unsigned xx = xr[j >> 1][j & 1];
#pragma unroll
    for (int d = 0; d < 4; ++d) {
      qa[2 * d] = dot2(perm_lo(wq[2 * j][d], wq[2 * j + 1][d]), xx, qa[2 * d]);
      qa[2 * d + 1] = dot2(perm_hi(wq[2 * j][d], wq[2 * j + 1][d]), xx, qa[2 * d + 1]);
    }
  }
  __builtin_amdgcn_sched_barrier(0);
  {
    float f0 = fold32(qa[0], qa[4]), f1 = fold32(qa[1], qa[5]), f2 = fold32(qa[2], qa[6]), f3 = fold32(qa[3], qa[7]);
    const float e0 = fold16(f0, f2), e1 = fold16(f1, f3);
    *reinterpret_cast<f32x2*>(&lds[L_Q + w * 128 + 8 * c + 2 * (g & 1) + 4 * (g >> 1)]) = f32x2{e0, e1};
  }
  STAMP(1);
  __syncthreads();
  u32x4 q;
  {
    f32x4 s0 = {0.f, 0.f, 0.f, 0.f}, s1 = s0;
#pragma unroll
    for (int ww = 0; ww < NWAVE; ++ww) {
      s0 += *reinterpret_cast<const f32x4*>(&lds[L_Q + ww * 128 + 8 * c]);
      s1 += *reinterpret_cast<const f32x4*>(&lds[L_Q + ww * 128 + 8 * c + 4]);
    }
    q = u32x4{cvtpk(s0[0], s0[1]), cvtpk(s0[2], s0[3]), cvtpk(s1[0], s1[1]), cvtpk(s1[2], s1[3])};
  }
  STAMP(2);

  float* scw = &lds[L_SC + (w * 4 + g) * NSTEP];
  float mx = -INFINITY;
#pragma unroll 1
  for (int it = 0; it < NBODY; ++it) {
    const bool last = it == NBODY - 1;
    const auto nr = last ? vr : kr;
    float x[4];
#pragma unroll
    for (int t = 0; t < 4; ++t) {
      float d = dot2(ring[t][0], q[0], 0.f);
      d = dot2(ring[t][1], q[1], d);
      d = dot2(ring[t][2], q[2], d);
      d = dot2(ring[t][3], q[3], d);
      x[t] = d;
      asm volatile("" : "+v"(x[t]));
      __builtin_amdgcn_sched_barrier(0);
      ring[t] = BLD(nr, kv_vo, SOFF(4 * it + 4 + t), AUX_NT);
      __builtin_amdgcn_sched_barrier(0);
    }
    const float a0 = x[0] + dpp<XOR8>(x[0]), a1 = x[1] + dpp<XOR8>(x[1]), a2 = x[2] + dpp<XOR8>(x[2]), a3 = x[3] + dpp<XOR8>(x[3]);
    const float b0 = (c & 8) ? a2 : a0, b1 = (c & 8) ? a3 : a1;
    const float c0 = b0 + dpp<XOR7>(b0), c1 = b1 + dpp<XOR7>(b1);
    float s = (c & 4) ? c1 : c0;
    s += dpp<XOR2>(s);
    s += dpp<XOR1>(s);
    mx = fmaxf(mx, s);
    scw[4 * it + (c >> 2)] = s;
    __builtin_amdgcn_sched_barrier(0);
  }
  STAMP(3);
  constexpr float C2 = 0.088388347648318440f * 1.4426950408889634f;
  mx = fmaxf(mx, dpp<XOR8>(mx));
  mx = fmaxf(mx, dpp<XOR7>(mx));
  mx = xrow_max(mx);
  float* red = lds + L_RED + 64;
  if (lane == 0) red[w] = mx;
  __syncthreads();
  {
    const f32x4 m0 = *reinterpret_cast<const f32x4*>(red), m1 = *reinterpret_cast<const f32x4*>(red + 4);
    mx = fmaxf(fmaxf(fmaxf(m0[0], m0[1]), fmaxf(m0[2], m0[3])), fmaxf(fmaxf(m1[0], m1[1]), fmaxf(m1[2], m1[3])));
  }
  const float mneg = -mx * C2;
#pragma unroll
  for (int cb = 0; cb < 5; ++cb)
#pragma unroll
    for (int jj = 0; jj < 2; ++jj)
#pragma unroll
      for (int d = 0; d < 4; ++d) {
        const unsigned r0 = wo[cb * 4 + 2 * jj][d], r1 = wo[cb * 4 + 2 * jj + 1][d];
        wo[cb * 4 + 2 * jj][d] = perm_lo(r0, r1);
        wo[cb * 4 + 2 * jj + 1][d] = perm_hi(r0, r1);
      }
#pragma unroll
  for (int i = 0; i < 20; ++i) asm volatile("" : "+v"(wo[i]));
  __builtin_amdgcn_sched_barrier(0);

  f32x2 acc2[4];
  float ls = 0.f;
#pragma unroll
  for (int i = 0; i < 4; ++i) acc2[i] = f32x2{0.f, 0.f};
#define VBODY(IT, LOAD)                                                                     \
  {                                                                                         \
    const f32x4 s4 = *reinterpret_cast<const f32x4*>(scw + 4 * (IT));                       \
    float p[4];                                                                             \
    _Pragma("unroll") for (int t = 0; t < 4; ++t) { p[t] = __builtin_amdgcn_exp2f(fmaf(s4[t], C2, mneg)); ls += p[t]; } \
    __builtin_amdgcn_sched_barrier(0);                                                      \
    _Pragma("unroll") for (int t = 0; t < 4; ++t) {                                         \
      _Pragma("unroll") for (int d = 0; d < 4; ++d)                                         \
        acc2[d] = __builtin_elementwise_fma(f32x2{__uint_as_float(ring[t][d] << 16), __uint_as_float(ring[t][d] & 0xffff0000u)}, f32x2{p[t], p[t]}, acc2[d]); \
      asm volatile("" : "+v"(acc2[0]), "+v"(acc2[1]), "+v"(acc2[2]), "+v"(acc2[3]));       \
      __builtin_amdgcn_sched_barrier(0);                                                    \
      if (LOAD) ring[t] = BLD(vr, kv_vo, SOFF(4 * (IT) + 4 + t), AUX_NT);                   \
      __builtin_amdgcn_sched_barrier(0);                                                    \
    }                                                                                       \
  }
#pragma unroll 1
  for (int it = 0; it < NBODY - 1; ++it) VBODY(it, true)
  VBODY(NBODY - 1, false)
#undef VBODY
  STAMP(4);
  {
    float f0 = fold32(acc2[0][0], acc2[2][0]), f1 = fold32(acc2[0][1], acc2[2][1]), f2 = fold32(acc2[1][0], acc2[3][0]), f3 = fold32(acc2[1][1], acc2[3][1]);
    const float e0 = fold16(f0, f2), e1 = fold16(f1, f3);
    *reinterpret_cast<f32x2*>(&lds[L_SC + w * 256 + 8 * c + 2 * (g & 1) + 4 * (g >> 1)]) = f32x2{e0, e1};
    ls = xrow_sum(ls);
    if (lane == 0) red[8 + w] = ls;
  }
  __syncthreads();
  f32x4 o4 = {0.f, 0.f, 0.f, 0.f};
#pragma unroll
  for (int ww = 0; ww < NWAVE; ++ww) o4 += *reinterpret_cast<const f32x4*>(&lds[L_SC + ww * 256 + 16 * w + 4 * g]);
  float lsum;
  {
    const f32x4 l0 = *reinterpret_cast<const f32x4*>(red + 8), l1 = *reinterpret_cast<const f32x4*>(red + 12);
    lsum = ((l0[0] + l0[1]) + (l0[2] + l0[3])) + ((l1[0] + l1[1]) + (l1[2] + l1[3]));
  }
  const float inv = 1.f / lsum;
  const unsigned o01 = cvtpk(o4[0] * inv, o4[1] * inv), o23 = cvtpk(o4[2] * inv, o4[3] * inv);
#pragma unroll
  for (int cb = 0; cb < 5; ++cb) {
    float y[8];
#pragma unroll
    for (int d = 0; d < 4; ++d) {
      y[2 * d] = dot2(wo[cb * 4 + 2][d], o23, dot2(wo[cb * 4][d], o01, 0.f));
      y[2 * d + 1] = dot2(wo[cb * 4 + 3][d], o23, dot2(wo[cb * 4 + 1][d], o01, 0.f));
    }
    float f0 = fold32(y[0], y[4]), f1 = fold32(y[1], y[5]), f2 = fold32(y[2], y[6]), f3 = fold32(y[3], y[7]);
    const float e0 = fold16(f0, f2), e1 = fold16(f1, f3);
    *reinterpret_cast<f32x2*>(&lds[L_Q + w * 640 + cb * 128 + 8 * c + 2 * (g & 1) + 4 * (g >> 1)]) = f32x2{e0, e1};
  }
  __syncthreads();
  const int row = br * NB + b, pi = h * NSPLIT + sp;
  if (tid < 160) {
    f32x4 s = {0.f, 0.f, 0.f, 0.f};
#pragma unroll
    for (int ww = 0; ww < NWAVE; ++ww) s += *reinterpret_cast<const f32x4*>(&lds[L_Q + ww * 640 + 4 * tid]);
    *reinterpret_cast<f32x4*>(a.part + ((size_t)row * NPART + pi) * DM + 4 * tid) = s;
  }
  if (tid == 0) *reinterpret_cast<f32x2*>(a.ml + (row * NPART + pi) * 2) = f32x2{mx * C2, lsum};
  STAMP(5);
}

__global__ void __launch_bounds__(256) attn_reduce(Args a) {
  __shared__ __attribute__((aligned(16))) float red[16 * 64];
  const int tid = threadIdx.x, cq = tid & 15, pg = tid >> 4;
  const int row = blockIdx.y, col0 = blockIdx.x * 64;
  f32x4 v[4];
#pragma unroll
  for (int i = 0; i < 4; ++i) v[i] = *reinterpret_cast<const f32x4*>(a.part + ((size_t)row * NPART + pg * 4 + i) * DM + col0 + 4 * cq);
  f32x4 s = {0.f, 0.f, 0.f, 0.f};
#pragma unroll
  for (int hh = 0; hh < 2; ++hh) {
    const f32x4 ml = *reinterpret_cast<const f32x4*>(a.ml + (row * NPART + pg * 4 + 2 * hh) * 2);
    const float M = fmaxf(ml[0], ml[2]);
    const float w0 = ml[1] * __builtin_amdgcn_exp2f(ml[0] - M), w1 = ml[3] * __builtin_amdgcn_exp2f(ml[2] - M);
    const float inv = 1.f / (w0 + w1);
    s += v[2 * hh] * (w0 * inv) + v[2 * hh + 1] * (w1 * inv);
  }
  *reinterpret_cast<f32x4*>(&red[pg * 64 + 4 * cq]) = s;
  __syncthreads();
  if (tid < 64) {
    float t = 0.f;
#pragma unroll
    for (int i = 0; i < 16; ++i) t += red[i * 64 + tid];
    const int br = row >> 1, b = row & 1;
    a.out[b * (2 * DM) + br * DM + col0 + tid] = __float2bfloat16(t);
  }
}

}

extern "C" void kernel_launch(void* const* d_in, const int* in_sizes, int n_in, void* d_out, int out_size, void* d_ws,
                              size_t ws_size, hipStream_t stream) {
  Args a;
  for (int br = 0; br < 2; ++br) {
    a.x[br] = (const char*)d_in[5 * br + 0];
    a.k[br] = (const char*)d_in[5 * br + 1];
    a.v[br] = (const char*)d_in[5 * br + 2];
    a.wq[br] = (const char*)d_in[5 * br + 3];
    a.wo[br] = (const char*)d_in[5 * br + 4];
  }
  a.part = (float*)d_ws;
  a.ml = (float*)((char*)d_ws + (size_t)4 * NPART * DM * 4);
  a.out = (__hip_bfloat16*)d_out;
  a.ts = (unsigned long long*)((char*)d_ws + (4 << 20));
  hipLaunchKernelGGL(attn_main, dim3(NWG), dim3(NTHR), 0, stream, a);
  hipLaunchKernelGGL(attn_reduce, dim3(10, 4), dim3(256), 0, stream, a);
}
```

```cpp
#include <hip/hip_runtime.h>
#include <hip/hip_bf16.h>
#include <stdint.h>

#define FLIGHT 4
#define AUX_W 2
#define ROTMUL 1
#define STAMPS 0
#define STAMPW 0
#define PSTAG 6
#define NPOLL 1
#define YRED 1
#define ADAPT 1
#define TAU 6800
#define T_OFF 120
#define DELTA 64
#define BAR_MID 1
#define K_INIT FLIGHT
#define WO_LAST 0
#define QPOLL_AT 12

namespace {

constexpr int DM = 640, NH = 32, SK = 4096, DH = 128, NB = 2;
constexpr int NSPLIT = 2;
constexpr int NWAVE = 8, NTHR = NWAVE * 64;
constexpr int KEYS_WG = SK / NSPLIT;
constexpr int NSTEP = KEYS_WG / (4 * NWAVE);
constexpr int STEP_B = NWAVE * 1024;
constexpr int RING = 16;
constexpr int NBODY = NSTEP / RING;
constexpr int NWG = 2 * NB * NH * NSPLIT;
constexpr int NPART = NH * NSPLIT;
constexpr int AUX_NT = 2, AUX_SC1 = 16;
constexpr unsigned SPIN_CAP = 1u << 16;

constexpr int WS_EPOCH = 0;
constexpr int WS_Q = 4096;
constexpr int WS_ML = WS_Q + 64 * 2 * 32 * 16;
constexpr int WS_O = WS_ML + 256 * 128;
constexpr int WS_Y = WS_O + 64 * 4 * 64 * 16;
constexpr int WS_END = WS_Y + 2 * 32 * 4 * 2 * 80 * 16;

typedef unsigned u32x4 __attribute__((ext_vector_type(4)));
typedef unsigned u32x3 __attribute__((ext_vector_type(3)));
typedef unsigned u32x2 __attribute__((ext_vector_type(2)));
typedef float f32x4 __attribute__((ext_vector_type(4)));
typedef float f32x2 __attribute__((ext_vector_type(2)));
typedef __bf16 bf16x2 __attribute__((ext_vector_type(2)));

__device__ __forceinline__ float dot2(unsigned a, unsigned b, float acc) {
  return __builtin_amdgcn_fdot2_f32_bf16(__builtin_bit_cast(bf16x2, a), __builtin_bit_cast(bf16x2, b), acc, false);
}
__device__ __forceinline__ unsigned cvtpk(float lo, float hi) {
  bf16x2 r;
  r[0] = (__bf16)lo;
  r[1] = (__bf16)hi;
  return __builtin_bit_cast(unsigned, r);
}
template <int CTRL> __device__ __forceinline__ float dpp(float x) {
  return __builtin_bit_cast(float, __builtin_amdgcn_mov_dpp(__builtin_bit_cast(int, x), CTRL, 0xf, 0xf, true));
}
constexpr int XOR1 = 0xB1, XOR2 = 0x4E, XOR7 = 0x141, ROR8 = 0x128, ROR4 = 0x124;
__device__ __forceinline__ unsigned perm_lo(unsigned a, unsigned b) { return __builtin_amdgcn_perm(b, a, 0x05040100u); }
__device__ __forceinline__ unsigned perm_hi(unsigned a, unsigned b) { return __builtin_amdgcn_perm(b, a, 0x07060302u); }
__device__ __forceinline__ float bf_lo(unsigned u) { return __uint_as_float(u << 16); }
__device__ __forceinline__ float bf_hi(unsigned u) { return __uint_as_float(u & 0xffff0000u); }

__device__ __forceinline__ float xrow_max(float x) {
  auto s = __builtin_amdgcn_permlane16_swap(__float_as_uint(x), __float_as_uint(x), false, false);
  x = fmaxf(__uint_as_float(s[0]), __uint_as_float(s[1]));
  auto t = __builtin_amdgcn_permlane32_swap(__float_as_uint(x), __float_as_uint(x), false, false);
  return fmaxf(__uint_as_float(t[0]), __uint_as_float(t[1]));
}
__device__ __forceinline__ float fold32(float a, float b) {
  auto t = __builtin_amdgcn_permlane32_swap(__float_as_uint(a), __float_as_uint(b), false, false);
  return __uint_as_float(t[0]) + __uint_as_float(t[1]);
}
__device__ __forceinline__ float fold16(float a, float b) {
  auto t = __builtin_amdgcn_permlane16_swap(__float_as_uint(a), __float_as_uint(b), false, false);
  return __uint_as_float(t[0]) + __uint_as_float(t[1]);
}
__device__ __forceinline__ float wave_sum(float x) {
  x += dpp<XOR1>(x); x += dpp<XOR2>(x); x += dpp<XOR7>(x); x += dpp<ROR8>(x);
  x = fold16(x, x);
  return fold32(x, x);
}
template <int N> __device__ __forceinline__ void wait_vm() {
  static_assert(N >= 0 && N < 64, "");
  __builtin_amdgcn_s_waitcnt((N & 15) | (7 << 4) | (15 << 8) | ((N >> 4) << 14));
}

__device__ __forceinline__ void wait_lvl(int lvl) {
  asm volatile(
      "s_waitcnt vmcnt(7)\n\t"
      "s_cmp_gt_i32 %0, 4\n\ts_cbranch_scc1 .Lw%=\n\ts_waitcnt vmcnt(6)\n\t"
      "s_cmp_gt_i32 %0, 3\n\ts_cbranch_scc1 .Lw%=\n\ts_waitcnt vmcnt(5)\n\t"
      "s_cmp_gt_i32 %0, 2\n\ts_cbranch_scc1 .Lw%=\n\ts_waitcnt vmcnt(4)\n\t"
      "s_cmp_gt_i32 %0, 1\n\ts_cbranch_scc1 .Lw%=\n\ts_waitcnt vmcnt(3)\n\t"
      "s_cmp_gt_i32 %0, 0\n\ts_cbranch_scc1 .Lw%=\n\ts_waitcnt vmcnt(2)\n"
      ".Lw%=:"
      :: "s"(lvl) : "scc", "memory");
}

struct Args {
  const char* x[2];
  const char* k[2];
  const char* v[2];
  const char* wq[2];
  const char* wo[2];
  char* ws;
  unsigned short* out;
};

#define RSRC(p, n) __builtin_amdgcn_make_buffer_rsrc((void*)(p), 0, (n), 0x00020000)
#define BLD(r, vo, so, aux) __builtin_amdgcn_raw_buffer_load_b128((r), (vo), (so), (aux))
#define SB() __builtin_amdgcn_sched_barrier(0)
#define WAVE_FENCE() __builtin_amdgcn_fence(__ATOMIC_ACQ_REL, "wavefront")
#define STAMP(i)

constexpr int L_SC = 0;
constexpr int L_Y = 2048;
constexpr int L_ML = L_Y + 8 * 320;
constexpr int L_OB = L_ML + 16;
constexpr int L_CNT = L_OB + 8 * 16;
constexpr int L_TOT = 21 * 1024;
static_assert(L_CNT + 4 <= L_TOT, "");

__device__ __forceinline__ bool last_arriver(float* lds, int i) {
  unsigned old = 0;
  if ((threadIdx.x & 63) == 0) old = __hip_atomic_fetch_add(reinterpret_cast<unsigned*>(lds + L_CNT) + i, 1u, __ATOMIC_ACQ_REL, __HIP_MEMORY_SCOPE_WORKGROUP);
  return __builtin_amdgcn_readfirstlane(old) == NWAVE - 1;
}

__global__ void __launch_bounds__(NTHR, 2) attn_fused(Args a) {
  __shared__ __attribute__((aligned(16))) float lds[L_TOT];
  const int tid = threadIdx.x, lane = tid & 63, w = __builtin_amdgcn_readfirstlane(tid >> 6);
  const int g = lane >> 4, c = lane & 15;
  STAMP(0);
  const unsigned t_start = (unsigned)__builtin_amdgcn_s_memrealtime();
  if (tid < 4) reinterpret_cast<unsigned*>(lds + L_CNT)[tid] = 0u;
  __syncthreads();
  SB();
  const char *px0 = a.x[0], *px1 = a.x[1], *pk0 = a.k[0], *pk1 = a.k[1], *pv0 = a.v[0], *pv1 = a.v[1], *pq0 = a.wq[0], *pq1 = a.wq[1], *po0 = a.wo[0], *po1 = a.wo[1];
  char* pws = a.ws;
  unsigned short* pout = a.out;
  asm volatile("" : "+s"(px0), "+s"(px1), "+s"(pk0), "+s"(pk1), "+s"(pv0), "+s"(pv1), "+s"(pq0), "+s"(pq1), "+s"(po0), "+s"(po1), "+s"(pws), "+s"(pout));
  const int bid = blockIdx.x, xcd = bid & 7, idx = bid >> 3;
  const int grp = xcd * 8 + (idx >> 2), mem = idx & 3;
  const int br = grp >> 5, h = grp & 31, b = mem >> 1, sp = mem & 1;
  const int wgi = grp * 4 + mem;
  const int rot = (wgi * ROTMUL) & (NSTEP - 1);
  const auto wsr = RSRC(pws, WS_END);
  int q_so = WS_Q + grp * 1024, ml_so = WS_ML + (wgi & ~1) * 128, o_so = WS_O + grp * 4096, y_so = WS_Y + br * (32 * 4 * 2 * 80 * 16);
  asm volatile("" : "+s"(q_so), "+s"(ml_so), "+s"(o_so), "+s"(y_so));

  const unsigned epoch_v = __builtin_amdgcn_raw_buffer_load_b32(wsr, 0, WS_EPOCH + br * 256, AUX_NT);
  const int cc = lane & 3, rg = lane >> 2;
  const int row0 = 80 * w + 5 * rg;
  u32x3 xr[2];
  {
    const auto xrs = RSRC(br ? px1 : px0, NB * DM * 2);
#pragma unroll
    for (int bb = 0; bb < 2; ++bb) xr[bb] = __builtin_amdgcn_raw_buffer_load_b96(xrs, (row0 * 2) & ~3, bb * DM * 2, AUX_NT);
  }
  const auto wqr = RSRC(br ? pq1 : pq0, DM * NH * DH * 2);
  u32x4 wq[5];
#pragma unroll
  for (int j = 0; j < 5; ++j) wq[j] = BLD(wqr, row0 * (NH * DH * 2) + 16 * cc, (h * DH + mem * 32) * 2 + j * (NH * DH * 2), AUX_NT);
  SB();
  __builtin_amdgcn_s_barrier();
  SB();
  const size_t kvoff = ((size_t)(b * NH + h) * SK + sp * KEYS_WG) * (DH * 2);
  const auto kr = RSRC((br ? pk1 : pk0) + kvoff, KEYS_WG * DH * 2);
  const auto vr = RSRC((br ? pv1 : pv0) + kvoff, KEYS_WG * DH * 2);
  const int kv_vo = w * 1024 + lane * 16;
#define SOFF(s) ((((s) + rot) & (NSTEP - 1)) * STEP_B)
  u32x4 ring[RING];
#pragma unroll
  for (int t = 0; t < K_INIT; ++t) ring[t] = BLD(kr, kv_vo, SOFF(t), AUX_NT);
  SB();
  const unsigned epoch = __builtin_amdgcn_readfirstlane(epoch_v);
  const unsigned tagA = epoch + 1u, tagB = epoch ^ 0x9E3779B9u;
#define TAG_OK(v) ((v)[1] == tagA && (v)[3] == tagB)

  {
    f32x2 qa[2][4];
#pragma unroll
    for (int bb = 0; bb < 2; ++bb) {
      const unsigned sh = (row0 & 1) * 16;
      const unsigned x01 = __builtin_amdgcn_alignbit(xr[bb][1], xr[bb][0], sh), x23 = __builtin_amdgcn_alignbit(xr[bb][2], xr[bb][1], sh), x4 = xr[bb][2] >> sh;
      const float xv[5] = {bf_lo(x01), bf_hi(x01), bf_lo(x23), bf_hi(x23), bf_lo(x4)};
#pragma unroll
      for (int d = 0; d < 4; ++d) {
        qa[bb][d] = f32x2{0.f, 0.f};
#pragma unroll
        for (int j = 0; j < 5; ++j) qa[bb][d] = __builtin_elementwise_fma(f32x2{bf_lo(wq[j][d]), bf_hi(wq[j][d])}, f32x2{xv[j], xv[j]}, qa[bb][d]);
      }
    }
    float f[8], e[4];
#pragma unroll
    for (int i = 0; i < 8; ++i) f[i] = fold32(qa[0][i >> 1][i & 1], qa[1][i >> 1][i & 1]);
#pragma unroll
    for (int i = 0; i < 4; ++i) { e[i] = fold16(f[i], f[i + 4]); e[i] += dpp<ROR8>(e[i]); e[i] += dpp<ROR4>(e[i]); }
    if (c < 4) *reinterpret_cast<f32x4*>(&lds[L_Y + w * 64 + (g >> 1) * 32 + 8 * c + 4 * (g & 1)]) = f32x4{e[0], e[1], e[2], e[3]};
  }
  if (last_arriver(lds, 0)) {
    if (lane < 16) {
      f32x4 s = {0.f, 0.f, 0.f, 0.f};
#pragma unroll
      for (int ww = 0; ww < NWAVE; ++ww) s += *reinterpret_cast<const f32x4*>(&lds[L_Y + ww * 64 + 4 * lane]);
      const u32x4 gr = {cvtpk(s[0], s[1]), tagA, cvtpk(s[2], s[3]), tagB};
      __builtin_amdgcn_raw_buffer_store_b128(gr, wsr, ((lane >> 3) * 32 + mem * 8 + (lane & 7)) * 16, q_so, AUX_SC1);
    }
  }
  STAMP(1);
  const auto wor = RSRC((br ? po1 : po0) + (size_t)h * DH * DM * 2, DH * DM * 2);
  const int wo_vo = (lane & 31) < 20 ? (16 * w + 8 * (lane >> 5)) * (DM * 2) + mem * 320 + 16 * (lane & 31) : 0x7fffff00;
  u32x4 wo[8];
  u32x4 qg0, qg1;
  const int q_vo = (b * 32 + 2 * c) * 16;
#pragma unroll
  for (int t = K_INIT; t < RING; ++t) {
    wait_vm<FLIGHT - 1>();
    ring[t] = BLD(kr, kv_vo, SOFF(t), AUX_NT);
    SB();
  }
#pragma unroll
  for (int j = 0; j < 8; ++j) {
    if (j == 8 - FLIGHT) { qg0 = BLD(wsr, q_vo, q_so, AUX_SC1); qg1 = BLD(wsr, q_vo + 16, q_so, AUX_SC1); }
    if (j >= 8 - FLIGHT) wait_vm<FLIGHT + 1>(); else wait_vm<FLIGHT - 1>();
    wo[j] = BLD(wor, wo_vo, j * (DM * 2), AUX_NT);
    SB();
  }
  if (!__all(TAG_OK(qg0) && TAG_OK(qg1))) {
#pragma unroll 1
    for (unsigned spins = 0; spins < SPIN_CAP; ++spins) {
      asm volatile("" ::: "memory");
      qg0 = BLD(wsr, q_vo, q_so, AUX_SC1); qg1 = BLD(wsr, q_vo + 16, q_so, AUX_SC1);
      if (__all(TAG_OK(qg0) && TAG_OK(qg1))) break;
      __builtin_amdgcn_s_sleep(1);
    }
  }
  const unsigned q0 = qg0[0], q1 = qg0[2], q2 = qg1[0], q3 = qg1[2];
  STAMP(2);

  int lvl;
#define RELAG(SLOT) { lvl = (int)((unsigned)__builtin_amdgcn_s_memrealtime() - t_start - (unsigned)(T_OFF + (((SLOT) * TAU) >> 8))) / DELTA; asm volatile("" : "+s"(lvl)); SB(); }
#define WAITW() wait_lvl(lvl)
  constexpr int SLOT0 = 5 + (WO_LAST ? 0 : 5) + RING;
  RELAG(SLOT0)
  float* scw = &lds[L_SC + (w * 4 + g) * NSTEP];
  float mx = -INFINITY;
#define KSTEP(T)                                                                        \
  {                                                                                     \
    float d = dot2(ring[T][0], q0, 0.f);                                                \
    d = dot2(ring[T][1], q1, d);                                                        \
    d = dot2(ring[T][2], q2, d);                                                        \
    d = dot2(ring[T][3], q3, d);                                                        \
    x[(T) & 3] = d;                                                                     \
    asm volatile("" : "+v"(x[(T) & 3]));                                                \
    SB();                                                                               \
    WAITW();                                                                            \
    ring[T] = BLD(nr, kv_vo, SOFF(RING * it + RING + (T)), AUX_NT);                     \
    SB();                                                                               \
    if (((T) & 3) == 3) {                                                               \
      const float a0 = x[0] + dpp<ROR8>(x[0]), a1 = x[1] + dpp<ROR8>(x[1]), a2 = x[2] + dpp<ROR8>(x[2]), a3 = x[3] + dpp<ROR8>(x[3]); \
      const float b0 = (c & 8) ? a2 : a0, b1 = (c & 8) ? a3 : a1;                       \
      const float c0 = b0 + dpp<XOR7>(b0), c1 = b1 + dpp<XOR7>(b1);                     \
      float s = (c & 4) ? c1 : c0;                                                      \
      s += dpp<XOR2>(s);                                                                \
      s += dpp<XOR1>(s);                                                                \
      mx = fmaxf(mx, s);                                                                \
      scw[RING * it + (T) - 3 + (c >> 2)] = s;                                          \
      SB();                                                                             \
      RELAG(SLOT0 + RING * it + (T) + 1)                                                \
    }                                                                                   \
  }
#pragma unroll 1
  for (int it = 0; it < NBODY; ++it) {
    const auto nr = (it == NBODY - 1) ? vr : kr;
    float x[4];
    KSTEP(0) KSTEP(1) KSTEP(2) KSTEP(3) KSTEP(4) KSTEP(5) KSTEP(6) KSTEP(7)
    KSTEP(8) KSTEP(9) KSTEP(10) KSTEP(11) KSTEP(12) KSTEP(13) KSTEP(14) KSTEP(15)
  }
#undef KSTEP
  STAMP(3);
  constexpr float C2 = 0.088388347648318440f * 1.4426950408889634f;
  mx = fmaxf(mx, dpp<ROR8>(mx));
  mx = fmaxf(mx, dpp<XOR7>(mx));
  mx = xrow_max(mx) * C2;
  WAVE_FENCE();
  {
    f32x4 s4 = *reinterpret_cast<const f32x4*>(scw + 4 * c);
#pragma unroll
    for (int t = 0; t < 4; ++t) s4[t] = __builtin_amdgcn_exp2f(fmaf(s4[t], C2, -mx));
    *reinterpret_cast<f32x4*>(scw + 4 * c) = s4;
    const float ls = wave_sum((s4[0] + s4[1]) + (s4[2] + s4[3]));
    if (lane == 0) *reinterpret_cast<f32x2*>(&lds[L_ML + 2 * w]) = f32x2{mx, ls};
  }
  if (last_arriver(lds, 1)) {
    f32x4 ml[4];
#pragma unroll
    for (int i = 0; i < 4; ++i) ml[i] = *reinterpret_cast<const f32x4*>(&lds[L_ML + 4 * i]);
    const float mwg = fmaxf(fmaxf(fmaxf(ml[0][0], ml[0][2]), fmaxf(ml[1][0], ml[1][2])), fmaxf(fmaxf(ml[2][0], ml[2][2]), fmaxf(ml[3][0], ml[3][2])));
    float lwg = 0.f;
#pragma unroll
    for (int i = 0; i < 4; ++i) lwg += ml[i][1] * __builtin_amdgcn_exp2f(ml[i][0] - mwg) + ml[i][3] * __builtin_amdgcn_exp2f(ml[i][2] - mwg);
    if (lane == 0) __builtin_amdgcn_raw_buffer_store_b128(u32x4{__float_as_uint(mwg), tagA, __float_as_uint(lwg), tagB}, wsr, sp * 128, ml_so, AUX_SC1);
  }
#pragma unroll
  for (int jj = 0; jj < 4; ++jj)
#pragma unroll
    for (int d = 0; d < 4; ++d) {
      const unsigned r0 = wo[2 * jj][d], r1 = wo[2 * jj + 1][d];
      wo[2 * jj][d] = perm_lo(r0, r1);
      wo[2 * jj + 1][d] = perm_hi(r0, r1);
    }
#pragma unroll
  for (int i = 0; i < 8; ++i) asm volatile("" : "+v"(wo[i]));
  SB();

  f32x2 acc2[4];
#pragma unroll
  for (int i = 0; i < 4; ++i) acc2[i] = f32x2{0.f, 0.f};
  u32x4 mlp;
  const int ml_vo = (lane & 1) * 128;
#define VSTEP(T, LOAD)                                                                  \
  {                                                                                     \
    if (((T) & 3) == 0) p4 = *reinterpret_cast<const f32x4*>(scw + RING * it + (T));    \
    _Pragma("unroll") for (int d = 0; d < 4; ++d)                                       \
      acc2[d] = __builtin_elementwise_fma(f32x2{bf_lo(ring[T][d]), bf_hi(ring[T][d])}, f32x2{p4[(T) & 3], p4[(T) & 3]}, acc2[d]); \
    asm volatile("" : "+v"(acc2[0]), "+v"(acc2[1]), "+v"(acc2[2]), "+v"(acc2[3]));     \
    SB();                                                                               \
    if (LOAD) { WAITW(); ring[T] = BLD(vr, kv_vo, SOFF(RING * it + RING + (T)), AUX_NT); } \
    SB();                                                                               \
    if (LOAD && ((T) & 3) == 3) RELAG(SLOT0 + NSTEP + RING * it + (T) + 1)              \
  }
#define VBODY(LOAD) { f32x4 p4;                                                         \
    VSTEP(0, LOAD) VSTEP(1, LOAD) VSTEP(2, LOAD) VSTEP(3, LOAD) VSTEP(4, LOAD) VSTEP(5, LOAD) VSTEP(6, LOAD) VSTEP(7, LOAD) \
    VSTEP(8, LOAD) VSTEP(9, LOAD) VSTEP(10, LOAD) VSTEP(11, LOAD) VSTEP(12, LOAD) VSTEP(13, LOAD) VSTEP(14, LOAD) VSTEP(15, LOAD) }
#pragma unroll 1
  for (int it = 0; it < NBODY - 1; ++it) {
    mlp = BLD(wsr, ml_vo, ml_so, AUX_SC1);
    SB();
    VBODY(true)
  }
  if (!__all(TAG_OK(mlp))) {
#pragma unroll 1
    for (unsigned spins = 0; spins < SPIN_CAP; ++spins) {
      asm volatile("" ::: "memory");
      mlp = BLD(wsr, ml_vo, ml_so, AUX_SC1);
      if (__all(TAG_OK(mlp))) break;
      __builtin_amdgcn_s_sleep(1);
    }
  }
  float scale;
  {
    const float m1 = __uint_as_float(mlp[0]), l1 = __uint_as_float(mlp[2]);
    const float M = fmaxf(m1, dpp<XOR1>(m1));
    const float t = l1 * __builtin_amdgcn_exp2f(m1 - M);
    scale = __builtin_amdgcn_exp2f(mx - M) / (t + dpp<XOR1>(t));
  }
  asm volatile("" : "+v"(scale));
  SB();
  { const int it = NBODY - 1; VBODY(false) }
#undef VBODY
#undef VSTEP
  WAVE_FENCE();
  STAMP(4);
  {
    const float f0 = fold32(acc2[0][0], acc2[2][0]), f1 = fold32(acc2[0][1], acc2[2][1]), f2 = fold32(acc2[1][0], acc2[3][0]), f3 = fold32(acc2[1][1], acc2[3][1]);
    const float e0 = fold16(f0, f2), e1 = fold16(f1, f3);
    *reinterpret_cast<f32x2*>(&lds[L_SC + w * 256 + 8 * c + 2 * (g & 1) + 4 * (g >> 1)]) = f32x2{e0 * scale, e1 * scale};
  }
  if (last_arriver(lds, 2)) {
    f32x2 s = {0.f, 0.f};
#pragma unroll
    for (int ww = 0; ww < NWAVE; ++ww) s += *reinterpret_cast<const f32x2*>(&lds[L_SC + ww * 256 + 2 * lane]);
    __builtin_amdgcn_raw_buffer_store_b128(u32x4{__float_as_uint(s[0]), tagA, __float_as_uint(s[1]), tagB}, wsr, mem * 1024 + lane * 16, o_so, AUX_SC1);
  }
  u32x4 keep0, keep1, keep2;
  {
    const int o_vo = ((lane >> 3) & 3) * 1024 + (8 * w + (lane & 7)) * 16;
    unsigned r0 = 0, r1 = 0;
    u32x4 og = BLD(wsr, o_vo, o_so, AUX_SC1);
    SB();
#pragma unroll 1
    for (unsigned spins = 0; spins < SPIN_CAP; ++spins) {
      if (__all(TAG_OK(og))) break;
      asm volatile("" ::: "memory");
      og = BLD(wsr, o_vo, o_so, AUX_SC1);
    }
    r0 = og[0]; r1 = og[2];
    float o0 = __uint_as_float(r0), o1 = __uint_as_float(r1);
    o0 += dpp<ROR8>(o0);
    o1 += dpp<ROR8>(o1);
    if ((lane & 40) == 0) reinterpret_cast<unsigned*>(lds + L_OB)[w * 16 + (lane >> 4) * 8 + (lane & 7)] = cvtpk(o0, o1);
  }
  WAVE_FENCE();
  STAMP(5);
  {
    const u32x4 oa = *reinterpret_cast<const u32x4*>(lds + L_OB + w * 16 + 4 * (lane >> 5));
    const u32x4 ob = *reinterpret_cast<const u32x4*>(lds + L_OB + w * 16 + 8 + 4 * (lane >> 5));
    float y[8];
#pragma unroll
    for (int i = 0; i < 8; ++i) {
      float ya = 0.f, yb = 0.f;
#pragma unroll
      for (int jj = 0; jj < 4; ++jj) { ya = dot2(wo[2 * jj + (i & 1)][i >> 1], oa[jj], ya); yb = dot2(wo[2 * jj + (i & 1)][i >> 1], ob[jj], yb); }
      y[i] = fold32(ya, yb);
    }
    if ((lane & 31) < 20) {
      float* yp = &lds[L_Y + w * 320 + (lane >> 5) * 160 + 8 * (lane & 31)];
      *reinterpret_cast<f32x4*>(yp) = f32x4{y[0], y[1], y[2], y[3]};
      *reinterpret_cast<f32x4*>(yp + 4) = f32x4{y[4], y[5], y[6], y[7]};
    }
  }
  __syncthreads();
  if (tid < 160) {
    f32x2 s = {0.f, 0.f};
#pragma unroll
    for (int ww = 0; ww < NWAVE; ++ww) s += *reinterpret_cast<const f32x2*>(&lds[L_Y + ww * 320 + 2 * tid]);
    __builtin_amdgcn_raw_buffer_store_b128(u32x4{__float_as_uint(s[0]), tagA, __float_as_uint(s[1]), tagB}, wsr, (h * 4 + mem) * 2560 + tid * 16, y_so, AUX_SC1);
  }
  STAMP(6);
  const int unit = wgi & 127;
  if (w == 3 && unit < 80) {
    const int y_vo = (lane >> 3) * (4 * 2560) + unit * 128 + (lane & 7) * 16;
    float s0 = 0.f, s1 = 0.f;
    u32x4 yg[4];
#pragma unroll
    for (int i = 0; i < 4; ++i) yg[i] = BLD(wsr, y_vo + i * (8 * 4 * 2560), y_so, AUX_SC1);
#define YLOAD(Y) { asm volatile("" ::: "memory"); _Pragma("unroll") for (int i = 0; i < 4; ++i) Y[i] = BLD(wsr, y_vo + i * (8 * 4 * 2560), y_so, AUX_SC1); }
#define YOK(Y) __all(TAG_OK(Y[0]) && TAG_OK(Y[1]) && TAG_OK(Y[2]) && TAG_OK(Y[3]))
#pragma unroll 1
    for (unsigned spins = 0; spins < SPIN_CAP; ++spins) {
      if (YOK(yg)) break;
      YLOAD(yg)
    }
    s0 = (__uint_as_float(yg[0][0]) + __uint_as_float(yg[1][0])) + (__uint_as_float(yg[2][0]) + __uint_as_float(yg[3][0]));
    s1 = (__uint_as_float(yg[0][2]) + __uint_as_float(yg[1][2])) + (__uint_as_float(yg[2][2]) + __uint_as_float(yg[3][2]));
    float s = fold32(s0, s1);
    s = fold16(s, s);
    s += dpp<ROR8>(s);
    auto t = __builtin_amdgcn_permlane32_swap(__float_as_uint(s), __float_as_uint(s), false, false);
    const int quarter = unit / 20, ob = (unit / 10) & 1, col = quarter * 160 + (unit % 10) * 16 + 2 * lane;
    if (lane < 8) *reinterpret_cast<unsigned*>(pout + ob * (2 * DM) + br * DM + col) = cvtpk(__uint_as_float(t[0]), __uint_as_float(t[1]));
    if (unit == 0 && lane == 0) __builtin_amdgcn_raw_buffer_store_b32(epoch + 1u, wsr, 0, WS_EPOCH + br * 256, AUX_SC1);
  }
}

}

extern "C" void kernel_launch(void* const* d_in, const int* in_sizes, int n_in, void* d_out, int out_size, void* d_ws,
                              size_t ws_size, hipStream_t stream) {
  Args a;
  for (int br = 0; br < 2; ++br) {
    a.x[br] = (const char*)d_in[5 * br + 0];
    a.k[br] = (const char*)d_in[5 * br + 1];
    a.v[br] = (const char*)d_in[5 * br + 2];
    a.wq[br] = (const char*)d_in[5 * br + 3];
    a.wo[br] = (const char*)d_in[5 * br + 4];
  }
  a.ws = (char*)d_ws;
  a.out = (unsigned short*)d_out;
  hipLaunchKernelGGL(attn_fused, dim3(NWG), dim3(NTHR), 0, stream, a);
}
```

```cpp
#include <hip/hip_runtime.h>
#include <hip/hip_bf16.h>
#include <stdint.h>

#define STAMPS 0
#define STAMPW 0
#define TAU 7100
#define T_OFF 120
#define DSHIFT 6
#define LVL_MID 1
#define LVL_MIN 1
#define LVL_MAX 4
#define CALIB 0
#define RMASK 3
#define BOOST 0
#define YD 25
#define OD -1
#define YS 0
#define PRE 0
#define YAUX AUX_SC1
#define OAUX AUX_SC1
#define FLIGHT 4
#define K_INIT FLIGHT
#define WQBAR 1
#define ROTMUL 1

namespace {

constexpr int DM = 640, NH = 32, SK = 4096, DH = 128, NB = 2;
constexpr int NSPLIT = 2;
constexpr int NWAVE = 8, NTHR = NWAVE * 64;
constexpr int KEYS_WG = SK / NSPLIT;
constexpr int NSTEP = KEYS_WG / (4 * NWAVE);
constexpr int STEP_B = NWAVE * 1024;
constexpr int RING = 16;
constexpr int NBODY = NSTEP / RING;
constexpr int NWG = 2 * NB * NH * NSPLIT;
constexpr int NPART = NH * NSPLIT;
constexpr int AUX_NT = 2, AUX_SC1 = 16;
constexpr unsigned SPIN_CAP = 1u << 16;

constexpr int WS_EPOCH = 0;
constexpr int WS_TS = 1024;
constexpr int WS_Q = 8192;
constexpr int WS_ML = WS_Q + 64 * 2 * 32 * 16;
constexpr int WS_O = WS_ML + 256 * 128;
constexpr int WS_Y = WS_O + 64 * 4 * 64 * 16;
constexpr int WS_END = WS_Y + 2 * 32 * 4 * 2 * 80 * 16;

typedef unsigned u32x4 __attribute__((ext_vector_type(4)));
typedef unsigned u32x3 __attribute__((ext_vector_type(3)));
typedef unsigned u32x2 __attribute__((ext_vector_type(2)));
typedef float f32x4 __attribute__((ext_vector_type(4)));
typedef float f32x2 __attribute__((ext_vector_type(2)));
typedef __bf16 bf16x2 __attribute__((ext_vector_type(2)));

__device__ __forceinline__ float dot2(unsigned a, unsigned b, float acc) {
  return __builtin_amdgcn_fdot2_f32_bf16(__builtin_bit_cast(bf16x2, a), __builtin_bit_cast(bf16x2, b), acc, false);
}
__device__ __forceinline__ unsigned cvtpk(float lo, float hi) {
  bf16x2 r;
  r[0] = (__bf16)lo;
  r[1] = (__bf16)hi;
  return __builtin_bit_cast(unsigned, r);
}
template <int CTRL> __device__ __forceinline__ float dpp(float x) {
  return __builtin_bit_cast(float, __builtin_amdgcn_mov_dpp(__builtin_bit_cast(int, x), CTRL, 0xf, 0xf, true));
}
constexpr int XOR1 = 0xB1, XOR2 = 0x4E, XOR7 = 0x141, ROR8 = 0x128, ROR4 = 0x124;
__device__ __forceinline__ unsigned perm_lo(unsigned a, unsigned b) { return __builtin_amdgcn_perm(b, a, 0x05040100u); }
__device__ __forceinline__ unsigned perm_hi(unsigned a, unsigned b) { return __builtin_amdgcn_perm(b, a, 0x07060302u); }
__device__ __forceinline__ float bf_lo(unsigned u) { return __uint_as_float(u << 16); }
__device__ __forceinline__ float bf_hi(unsigned u) { return __uint_as_float(u & 0xffff0000u); }

__device__ __forceinline__ float xrow_max(float x) {
  auto s = __builtin_amdgcn_permlane16_swap(__float_as_uint(x), __float_as_uint(x), false, false);
  x = fmaxf(__uint_as_float(s[0]), __uint_as_float(s[1]));
  auto t = __builtin_amdgcn_permlane32_swap(__float_as_uint(x), __float_as_uint(x), false, false);
  return fmaxf(__uint_as_float(t[0]), __uint_as_float(t[1]));
}
__device__ __forceinline__ float fold32(float a, float b) {
  auto t = __builtin_amdgcn_permlane32_swap(__float_as_uint(a), __float_as_uint(b), false, false);
  return __uint_as_float(t[0]) + __uint_as_float(t[1]);
}
__device__ __forceinline__ float fold16(float a, float b) {
  auto t = __builtin_amdgcn_permlane16_swap(__float_as_uint(a), __float_as_uint(b), false, false);
  return __uint_as_float(t[0]) + __uint_as_float(t[1]);
}
__device__ __forceinline__ float wave_sum(float x) {
  x += dpp<XOR1>(x); x += dpp<XOR2>(x); x += dpp<XOR7>(x); x += dpp<ROR8>(x);
  x = fold16(x, x);
  return fold32(x, x);
}
template <int N> __device__ __forceinline__ void wait_vm() {
  static_assert(N >= 0 && N < 64, "");
  __builtin_amdgcn_s_waitcnt((N & 15) | (7 << 4) | (15 << 8) | ((N >> 4) << 14));
}

__device__ __forceinline__ void wait_lvl(int lvl) {
  asm volatile(
      "s_waitcnt vmcnt(7)\n\t"
      "s_cmp_gt_i32 %0, 5\n\ts_cbranch_scc1 .Lw%=\n\ts_waitcnt vmcnt(6)\n\t"
      "s_cmp_gt_i32 %0, 4\n\ts_cbranch_scc1 .Lw%=\n\ts_waitcnt vmcnt(5)\n\t"
      "s_cmp_gt_i32 %0, 3\n\ts_cbranch_scc1 .Lw%=\n\ts_waitcnt vmcnt(4)\n\t"
      "s_cmp_gt_i32 %0, 2\n\ts_cbranch_scc1 .Lw%=\n\ts_waitcnt vmcnt(3)\n\t"
      "s_cmp_gt_i32 %0, 1\n\ts_cbranch_scc1 .Lw%=\n\ts_waitcnt vmcnt(2)\n\t"
      "s_cmp_gt_i32 %0, 0\n\ts_cbranch_scc1 .Lw%=\n\ts_waitcnt vmcnt(1)\n"
      ".Lw%=:"
      :: "s"(lvl) : "scc", "memory");
}
__device__ __forceinline__ unsigned now_ticks() { return (unsigned)__builtin_amdgcn_s_memrealtime(); }

struct Args {
  const char* x[2];
  const char* k[2];
  const char* v[2];
  const char* wq[2];
  const char* wo[2];
  char* ws;
  unsigned short* out;
};

#define RSRC(p, n) __builtin_amdgcn_make_buffer_rsrc((void*)(p), 0, (n), 0x00020000)
#define BLD(r, vo, so, aux) __builtin_amdgcn_raw_buffer_load_b128((r), (vo), (so), (aux))
#define ST128(v, vo, so) __builtin_amdgcn_raw_buffer_store_b128((v), wsr, (vo) + (so), 0, AUX_SC1)
#define SB() __builtin_amdgcn_sched_barrier(0)
#define WAVE_FENCE() __builtin_amdgcn_fence(__ATOMIC_ACQ_REL, "wavefront")
#define STAMP(i)

constexpr int L_SC = 0;
constexpr int L_Y = 2048;
constexpr int L_ML = L_Y + 8 * 320;
constexpr int L_OB = L_ML + 16;
constexpr int L_CNT = L_OB + 8 * 16;
constexpr int L_TOT = 21 * 1024;
static_assert(L_CNT + 4 <= L_TOT, "");

__device__ __forceinline__ bool last_arriver(float* lds, int i) {
  unsigned old = 0;
  if ((threadIdx.x & 63) == 0) old = __hip_atomic_fetch_add(reinterpret_cast<unsigned*>(lds + L_CNT) + i, 1u, __ATOMIC_ACQ_REL, __HIP_MEMORY_SCOPE_WORKGROUP);
  return __builtin_amdgcn_readfirstlane(old) == NWAVE - 1;
}

__global__ void __launch_bounds__(NTHR, 2) attn_fused(Args a) {
  __shared__ __attribute__((aligned(16))) float lds[L_TOT];
  const int tid = threadIdx.x, lane = tid & 63, w = __builtin_amdgcn_readfirstlane(tid >> 6);
  const int g = lane >> 4, c = lane & 15;
  STAMP(0);
  const unsigned t_start = now_ticks();
  if (tid < 4) reinterpret_cast<unsigned*>(lds + L_CNT)[tid] = 0u;
  __syncthreads();
  SB();
  const char *px0 = a.x[0], *px1 = a.x[1], *pk0 = a.k[0], *pk1 = a.k[1], *pv0 = a.v[0], *pv1 = a.v[1], *pq0 = a.wq[0], *pq1 = a.wq[1], *po0 = a.wo[0], *po1 = a.wo[1];
  char* pws = a.ws;
  unsigned short* pout = a.out;
  asm volatile("" : "+s"(px0), "+s"(px1), "+s"(pk0), "+s"(pk1), "+s"(pv0), "+s"(pv1), "+s"(pq0), "+s"(pq1), "+s"(po0), "+s"(po1), "+s"(pws), "+s"(pout));
  const int bid = blockIdx.x, xcd = bid & 7, idx = bid >> 3;
  const int grp = xcd * 8 + (idx >> 2), mem = idx & 3;
  const int br = grp >> 5, h = grp & 31, b = mem >> 1, sp = mem & 1;
  const int wgi = grp * 4 + mem;
  const int rot = (wgi * ROTMUL) & (NSTEP - 1);
  const auto wsr = RSRC(pws, WS_END);
  int q_so = WS_Q + grp * 1024, ml_so = WS_ML + (wgi & ~1) * 128, o_so = WS_O + grp * 4096, y_so = WS_Y + br * (32 * 4 * 2 * 80 * 16);
  asm volatile("" : "+s"(q_so), "+s"(ml_so), "+s"(o_so), "+s"(y_so));

  const unsigned epoch_v = __builtin_amdgcn_raw_buffer_load_b32(wsr, 0, WS_EPOCH + br * 256, AUX_NT);
  const unsigned epoch2_v = __builtin_amdgcn_raw_buffer_load_b32(wsr, (lane & 4) * 64, WS_EPOCH, AUX_NT);
  const int cc = lane & 3, rg = lane >> 2;
  const int row0 = 80 * w + 5 * rg;
  u32x3 xr[2];
  {
    const auto xrs = RSRC(br ? px1 : px0, NB * DM * 2);
#pragma unroll
    for (int bb = 0; bb < 2; ++bb) xr[bb] = __builtin_amdgcn_raw_buffer_load_b96(xrs, (row0 * 2) & ~3, bb * DM * 2, AUX_NT);
  }
  const auto wqr = RSRC(br ? pq1 : pq0, DM * NH * DH * 2);
  u32x4 wq[5];
#pragma unroll
  for (int j = 0; j < 5; ++j) wq[j] = BLD(wqr, row0 * (NH * DH * 2) + 16 * cc, (h * DH + mem * 32) * 2 + j * (NH * DH * 2), AUX_NT);
  SB();
  if (WQBAR) __builtin_amdgcn_s_barrier();
  SB();
  const size_t kvoff = ((size_t)(b * NH + h) * SK + sp * KEYS_WG) * (DH * 2);
  const auto kr = RSRC((br ? pk1 : pk0) + kvoff, KEYS_WG * DH * 2);
  const auto vr = RSRC((br ? pv1 : pv0) + kvoff, KEYS_WG * DH * 2);
  const int kv_vo = w * 1024 + lane * 16;
#define SOFF(s) ((((s) + rot) & (NSTEP - 1)) * STEP_B)
  u32x4 ring[RING];
#pragma unroll
  for (int t = 0; t < K_INIT; ++t) ring[t] = BLD(kr, kv_vo, SOFF(t), AUX_NT);
  SB();
  const unsigned epoch = __builtin_amdgcn_readfirstlane(epoch_v);
  const unsigned tagA = epoch + 1u, tagB = epoch ^ 0x9E3779B9u;
#define TAG_OK(v) ((v)[1] == tagA && (v)[3] == tagB)

  {
    f32x2 qa[2][4];
#pragma unroll
    for (int bb = 0; bb < 2; ++bb) {
      const unsigned sh = (row0 & 1) * 16;
      const unsigned x01 = __builtin_amdgcn_alignbit(xr[bb][1], xr[bb][0], sh), x23 = __builtin_amdgcn_alignbit(xr[bb][2], xr[bb][1], sh), x4 = xr[bb][2] >> sh;
      const float xv[5] = {bf_lo(x01), bf_hi(x01), bf_lo(x23), bf_hi(x23), bf_lo(x4)};
#pragma unroll
      for (int d = 0; d < 4; ++d) {
        qa[bb][d] = f32x2{0.f, 0.f};
#pragma unroll
        for (int j = 0; j < 5; ++j) qa[bb][d] = __builtin_elementwise_fma(f32x2{bf_lo(wq[j][d]), bf_hi(wq[j][d])}, f32x2{xv[j], xv[j]}, qa[bb][d]);
      }
    }
    float f[8], e[4];
#pragma unroll
    for (int i = 0; i < 8; ++i) f[i] = fold32(qa[0][i >> 1][i & 1], qa[1][i >> 1][i & 1]);
#pragma unroll
    for (int i = 0; i < 4; ++i) { e[i] = fold16(f[i], f[i + 4]); e[i] += dpp<ROR8>(e[i]); e[i] += dpp<ROR4>(e[i]); }
    if (c < 4) *reinterpret_cast<f32x4*>(&lds[L_Y + w * 64 + (g >> 1) * 32 + 8 * c + 4 * (g & 1)]) = f32x4{e[0], e[1], e[2], e[3]};
  }
  if (last_arriver(lds, 0)) {
    if (lane < 16) {
      f32x4 s = {0.f, 0.f, 0.f, 0.f};
#pragma unroll
      for (int ww = 0; ww < NWAVE; ++ww) s += *reinterpret_cast<const f32x4*>(&lds[L_Y + ww * 64 + 4 * lane]);
      const u32x4 gr = {cvtpk(s[0], s[1]), tagA, cvtpk(s[2], s[3]), tagB};
      ST128((gr), (((lane >> 3) * 32 + mem * 8 + (lane & 7)) * 16), q_so);
    }
  }
  STAMP(1);
  const auto wor = RSRC((br ? po1 : po0) + (size_t)h * DH * DM * 2, DH * DM * 2);
  const int wo_vo = (lane & 31) < 20 ? (16 * w + 8 * (lane >> 5)) * (DM * 2) + mem * 320 + 16 * (lane & 31) : 0x40000000;
  u32x4 wo[8];
  u32x4 qg0, qg1;
  const int q_vo = (b * 32 + 2 * c) * 16;
#pragma unroll
  for (int t = K_INIT; t < RING; ++t) {
    wait_vm<FLIGHT - 1>();
    ring[t] = BLD(kr, kv_vo, SOFF(t), AUX_NT);
    SB();
  }
#pragma unroll
  for (int j = 0; j < 8; ++j) {
    if (j == 8 - FLIGHT) { qg0 = BLD(wsr, q_vo, q_so, AUX_SC1); qg1 = BLD(wsr, q_vo + 16, q_so, AUX_SC1); }
    if (j >= 8 - FLIGHT) wait_vm<FLIGHT + 1>(); else wait_vm<FLIGHT - 1>();
    wo[j] = BLD(wor, wo_vo, j * (DM * 2), AUX_NT);
    SB();
  }
  if (!__all(TAG_OK(qg0) && TAG_OK(qg1))) {
#pragma unroll 1
    for (unsigned spins = 0; spins < SPIN_CAP; ++spins) {
      asm volatile("" ::: "memory");
      qg0 = BLD(wsr, q_vo, q_so, AUX_SC1); qg1 = BLD(wsr, q_vo + 16, q_so, AUX_SC1);
      if (__all(TAG_OK(qg0) && TAG_OK(qg1))) break;
      __builtin_amdgcn_s_sleep(1);
    }
  }
  const unsigned q0 = qg0[0], q1 = qg0[2], q2 = qg1[0], q3 = qg1[2];
  STAMP(2);

  constexpr int SLOT0 = 5 + 5 + RING;
  unsigned t_ref = t_start + T_OFF + ((SLOT0 * TAU) >> 8);
  int lvl, body = 0;
  u32x2 tsv = {0u, 0u};
  const unsigned ts_tag = epoch2_v + 1u;
#define RELAG(T) { lvl = ((int)(now_ticks() - t_ref - (unsigned)(((T) * TAU) >> 8)) >> DSHIFT) + LVL_MID;                     \
    asm volatile("s_max_i32 %0, %0, %1\n\ts_min_i32 %0, %0, %2" : "+s"(lvl) : "i"(LVL_MIN), "i"(LVL_MAX) : "scc"); SB(); }
#define BODY_BEGIN() RELAG(0)
#define BODY_MID() {}
#define BODY_END() { t_ref += (RING * TAU) >> 8; }

  float* scw = &lds[L_SC + (w * 4 + g) * NSTEP];
  float mx = -INFINITY;
#define KSTEP(T)                                                                        \
  {                                                                                     \
    float d = dot2(ring[T][0], q0, 0.f);                                                \
    d = dot2(ring[T][1], q1, d);                                                        \
    d = dot2(ring[T][2], q2, d);                                                        \
    d = dot2(ring[T][3], q3, d);                                                        \
    x[(T) & 3] = d;                                                                     \
    asm volatile("" : "+v"(x[(T) & 3]));                                                \
    SB();                                                                               \
    wait_lvl((BOOST && (T) >= 14) ? lvl + bst : lvl);                                   \
    ring[T] = BLD(nr, kv_vo, SOFF(RING * it + RING + (T)), AUX_NT);                     \
    SB();                                                                               \
    if (((T) & 3) == 3) {                                                               \
      const float a0 = x[0] + dpp<ROR8>(x[0]), a1 = x[1] + dpp<ROR8>(x[1]), a2 = x[2] + dpp<ROR8>(x[2]), a3 = x[3] + dpp<ROR8>(x[3]); \
      const float b0 = (c & 8) ? a2 : a0, b1 = (c & 8) ? a3 : a1;                       \
      const float c0 = b0 + dpp<XOR7>(b0), c1 = b1 + dpp<XOR7>(b1);                     \
      float s = (c & 4) ? c1 : c0;                                                      \
      s += dpp<XOR2>(s);                                                                \
      s += dpp<XOR1>(s);                                                                \
      mx = fmaxf(mx, s);                                                                \
      scw[RING * it + (T) - 3 + (c >> 2)] = s;                                          \
      SB();                                                                             \
    }                                                                                   \
    if ((T) == 9) BODY_MID()                                                            \
    if (((T) & RMASK) == RMASK && (T) != 15) RELAG((T) + 1)                             \
  }
#pragma unroll 1
  for (int it = 0; it < NBODY; ++it) {
    const auto nr = (it == NBODY - 1) ? vr : kr;
    float x[4];
    const int bst = (it == NBODY - 1) ? BOOST : 0;
    BODY_BEGIN()
    KSTEP(0) KSTEP(1) KSTEP(2) KSTEP(3) KSTEP(4) KSTEP(5) KSTEP(6) KSTEP(7)
    KSTEP(8) KSTEP(9) KSTEP(10) KSTEP(11) KSTEP(12) KSTEP(13) KSTEP(14) KSTEP(15)
    BODY_END()
  }
#undef KSTEP
  STAMP(3);
  constexpr float C2 = 0.088388347648318440f * 1.4426950408889634f;
  mx = fmaxf(mx, dpp<ROR8>(mx));
  mx = fmaxf(mx, dpp<XOR7>(mx));
  mx = xrow_max(mx) * C2;
  WAVE_FENCE();
  {
    f32x4 s4 = *reinterpret_cast<const f32x4*>(scw + 4 * c);
#pragma unroll
    for (int t = 0; t < 4; ++t) s4[t] = __builtin_amdgcn_exp2f(fmaf(s4[t], C2, -mx));
    *reinterpret_cast<f32x4*>(scw + 4 * c) = s4;
    const float ls = wave_sum((s4[0] + s4[1]) + (s4[2] + s4[3]));
    if (lane == 0) *reinterpret_cast<f32x2*>(&lds[L_ML + 2 * w]) = f32x2{mx, ls};
  }
  if (last_arriver(lds, 1)) {
    f32x4 ml[4];
#pragma unroll
    for (int i = 0; i < 4; ++i) ml[i] = *reinterpret_cast<const f32x4*>(&lds[L_ML + 4 * i]);
    const float mwg = fmaxf(fmaxf(fmaxf(ml[0][0], ml[0][2]), fmaxf(ml[1][0], ml[1][2])), fmaxf(fmaxf(ml[2][0], ml[2][2]), fmaxf(ml[3][0], ml[3][2])));
    float lwg = 0.f;
#pragma unroll
    for (int i = 0; i < 4; ++i) lwg += ml[i][1] * __builtin_amdgcn_exp2f(ml[i][0] - mwg) + ml[i][3] * __builtin_amdgcn_exp2f(ml[i][2] - mwg);
    if (lane == 0) ST128((u32x4{__float_as_uint(mwg), tagA, __float_as_uint(lwg), tagB}), (sp * 128), ml_so);
  }
#pragma unroll
  for (int jj = 0; jj < 4; ++jj)
#pragma unroll
    for (int d = 0; d < 4; ++d) {
      const unsigned r0 = wo[2 * jj][d], r1 = wo[2 * jj + 1][d];
      wo[2 * jj][d] = perm_lo(r0, r1);
      wo[2 * jj + 1][d] = perm_hi(r0, r1);
    }
#pragma unroll
  for (int i = 0; i < 8; ++i) asm volatile("" : "+v"(wo[i]));
  SB();

  u32x4 pre0 = {0u, 0u, 0u, 0u}, pre1 = pre0, pre2 = pre0, pre3 = pre0, pre4 = pre0;
  if (PRE & 1) {
    pre4 = BLD(wsr, ((lane >> 3) & 3) * 1024 + (8 * w + (lane & 7)) * 16, o_so, AUX_SC1);
    if (w == 3 && (wgi & 127) < 80) {
      const int y_vo = (lane >> 3) * (4 * 2560) + (wgi & 127) * 128 + (lane & 7) * 16;
      pre0 = BLD(wsr, y_vo, y_so, AUX_SC1); pre1 = BLD(wsr, y_vo + 8 * 4 * 2560, y_so, AUX_SC1);
      pre2 = BLD(wsr, y_vo + 16 * 4 * 2560, y_so, AUX_SC1); pre3 = BLD(wsr, y_vo + 24 * 4 * 2560, y_so, AUX_SC1);
    }
  }
  if (PRE & 2) {
    if (tid < 160) ST128((u32x4{0u, ~tagA, 0u, ~tagB}), (h * 4 + mem) * 2560 + tid * 16, y_so);
    if (w == 4) ST128((u32x4{0u, ~tagA, 0u, ~tagB}), mem * 1024 + lane * 16, o_so);
  }
  SB();
  f32x2 acc2[4];
#pragma unroll
  for (int i = 0; i < 4; ++i) acc2[i] = f32x2{0.f, 0.f};
  u32x4 mlp;
  const int ml_vo = (lane & 1) * 128;
#define VSTEP(T, LOAD)                                                                  \
  {                                                                                     \
    if (((T) & 3) == 0) p4 = *reinterpret_cast<const f32x4*>(scw + RING * it + (T));    \
    _Pragma("unroll") for (int d = 0; d < 4; ++d)                                       \
      acc2[d] = __builtin_elementwise_fma(f32x2{bf_lo(ring[T][d]), bf_hi(ring[T][d])}, f32x2{p4[(T) & 3], p4[(T) & 3]}, acc2[d]); \
    asm volatile("" : "+v"(acc2[0]), "+v"(acc2[1]), "+v"(acc2[2]), "+v"(acc2[3]));     \
    SB();                                                                               \
    if (LOAD) { wait_lvl(lvl); ring[T] = BLD(vr, kv_vo, SOFF(RING * it + RING + (T)), AUX_NT); } \
    SB();                                                                               \
    if (LOAD && (T) == 9) BODY_MID()                                                    \
    if (LOAD && ((T) & RMASK) == RMASK && (T) != 15) RELAG((T) + 1)                     \
  }
#define VBODY(LOAD) { f32x4 p4;                                                         \
    VSTEP(0, LOAD) VSTEP(1, LOAD) VSTEP(2, LOAD) VSTEP(3, LOAD) VSTEP(4, LOAD) VSTEP(5, LOAD) VSTEP(6, LOAD) VSTEP(7, LOAD) \
    VSTEP(8, LOAD) VSTEP(9, LOAD) VSTEP(10, LOAD) VSTEP(11, LOAD) VSTEP(12, LOAD) VSTEP(13, LOAD) VSTEP(14, LOAD) VSTEP(15, LOAD) }
#pragma unroll 1
  for (int it = 0; it < NBODY - 1; ++it) {
    mlp = BLD(wsr, ml_vo, ml_so, AUX_SC1);
    SB();
    BODY_BEGIN()
    VBODY(true)
    BODY_END()
  }
  if (!__all(TAG_OK(mlp))) {
#pragma unroll 1
    for (unsigned spins = 0; spins < SPIN_CAP; ++spins) {
      asm volatile("" ::: "memory");
      mlp = BLD(wsr, ml_vo, ml_so, AUX_SC1);
      if (__all(TAG_OK(mlp))) break;
      __builtin_amdgcn_s_sleep(1);
    }
  }
  float scale;
  {
    const float m1 = __uint_as_float(mlp[0]), l1 = __uint_as_float(mlp[2]);
    const float M = fmaxf(m1, dpp<XOR1>(m1));
    const float t = l1 * __builtin_amdgcn_exp2f(m1 - M);
    scale = __builtin_amdgcn_exp2f(mx - M) / (t + dpp<XOR1>(t));
  }
  asm volatile("" : "+v"(scale) : "v"(pre0), "v"(pre1), "v"(pre2), "v"(pre3), "v"(pre4));
  SB();
  { const int it = NBODY - 1; VBODY(false) }
#undef VBODY
#undef VSTEP
  WAVE_FENCE();
  STAMP(4);
  {
    const float f0 = fold32(acc2[0][0], acc2[2][0]), f1 = fold32(acc2[0][1], acc2[2][1]), f2 = fold32(acc2[1][0], acc2[3][0]), f3 = fold32(acc2[1][1], acc2[3][1]);
    const float e0 = fold16(f0, f2), e1 = fold16(f1, f3);
    *reinterpret_cast<f32x2*>(&lds[L_SC + w * 256 + 8 * c + 2 * (g & 1) + 4 * (g >> 1)]) = f32x2{e0 * scale, e1 * scale};
  }
  if (last_arriver(lds, 2)) {
    f32x2 s = {0.f, 0.f};
#pragma unroll
    for (int ww = 0; ww < NWAVE; ++ww) s += *reinterpret_cast<const f32x2*>(&lds[L_SC + ww * 256 + 2 * lane]);
    ST128((u32x4{__float_as_uint(s[0]), tagA, __float_as_uint(s[1]), tagB}), (mem * 1024 + lane * 16), o_so);
  }
  {
    const int o_vo = ((lane >> 3) & 3) * 1024 + (8 * w + (lane & 7)) * 16;
    if (OD >= 0) { __builtin_amdgcn_s_barrier(); if (OD > 0) __builtin_amdgcn_s_sleep(OD > 0 ? OD : 1); }
    u32x4 og;
#pragma unroll 1
    for (unsigned spins = 0; spins < SPIN_CAP; ++spins) {
      asm volatile("" ::: "memory");
      og = BLD(wsr, o_vo, o_so, OAUX);
      if (__all(TAG_OK(og))) break;
    }
    float o0 = __uint_as_float(og[0]), o1 = __uint_as_float(og[2]);
    o0 += dpp<ROR8>(o0);
    o1 += dpp<ROR8>(o1);
    if ((lane & 40) == 0) reinterpret_cast<unsigned*>(lds + L_OB)[w * 16 + (lane >> 4) * 8 + (lane & 7)] = cvtpk(o0, o1);
  }
  WAVE_FENCE();
  STAMP(5);
  {
    const u32x4 oa = *reinterpret_cast<const u32x4*>(lds + L_OB + w * 16 + 4 * (lane >> 5));
    const u32x4 ob = *reinterpret_cast<const u32x4*>(lds + L_OB + w * 16 + 8 + 4 * (lane >> 5));
    float y[8];
#pragma unroll
    for (int i = 0; i < 8; ++i) {
      float ya = 0.f, yb = 0.f;
#pragma unroll
      for (int jj = 0; jj < 4; ++jj) { ya = dot2(wo[2 * jj + (i & 1)][i >> 1], oa[jj], ya); yb = dot2(wo[2 * jj + (i & 1)][i >> 1], ob[jj], yb); }
      y[i] = fold32(ya, yb);
    }
    if ((lane & 31) < 20) {
      float* yp = &lds[L_Y + w * 320 + (lane >> 5) * 160 + 8 * (lane & 31)];
      *reinterpret_cast<f32x4*>(yp) = f32x4{y[0], y[1], y[2], y[3]};
      *reinterpret_cast<f32x4*>(yp + 4) = f32x4{y[4], y[5], y[6], y[7]};
    }
  }
  __syncthreads();
  if (tid < 160) {
    f32x2 s = {0.f, 0.f};
#pragma unroll
    for (int ww = 0; ww < NWAVE; ++ww) s += *reinterpret_cast<const f32x2*>(&lds[L_Y + ww * 320 + 2 * tid]);
    ST128((u32x4{__float_as_uint(s[0]), tagA, __float_as_uint(s[1]), tagB}), ((h * 4 + mem) * 2560 + tid * 16), y_so);
  }
  STAMP(6);
  const int unit = wgi & 127;
  if (w == 3 && unit < 80) {
    const int y_vo = (lane >> 3) * (4 * 2560) + unit * 128 + (lane & 7) * 16;
    if (YD > 0) __builtin_amdgcn_s_sleep(YD > 0 ? YD : 1);
    u32x4 yg[4];
#pragma unroll 1
    for (unsigned spins = 0; spins < SPIN_CAP; ++spins) {
      asm volatile("" ::: "memory");
#pragma unroll
      for (int i = 0; i < 4; ++i) yg[i] = BLD(wsr, y_vo + i * (8 * 4 * 2560), y_so, YAUX);
      unsigned bad = 0;
#pragma unroll
      for (int i = 0; i < 4; ++i) bad |= (yg[i][1] ^ tagA) | (yg[i][3] ^ tagB);
      if (__all(bad == 0)) break;
      if (YS > 0) __builtin_amdgcn_s_sleep(YS > 0 ? YS : 1);
    }
    const float s0 = (__uint_as_float(yg[0][0]) + __uint_as_float(yg[1][0])) + (__uint_as_float(yg[2][0]) + __uint_as_float(yg[3][0]));
    const float s1 = (__uint_as_float(yg[0][2]) + __uint_as_float(yg[1][2])) + (__uint_as_float(yg[2][2]) + __uint_as_float(yg[3][2]));
    float s = fold32(s0, s1);
    s = fold16(s, s);
    s += dpp<ROR8>(s);
    auto t = __builtin_amdgcn_permlane32_swap(__float_as_uint(s), __float_as_uint(s), false, false);
    const int col = (unit / 20) * 160 + (unit % 10) * 16 + 2 * lane;
    if (lane < 8) *reinterpret_cast<unsigned*>(pout + ((unit / 10) & 1) * (2 * DM) + br * DM + col) = cvtpk(__uint_as_float(t[0]), __uint_as_float(t[1]));
    if (unit == 0 && lane == 0) __builtin_amdgcn_raw_buffer_store_b32(epoch + 1u, wsr, 0, WS_EPOCH + br * 256, AUX_SC1);
  }
}

}

extern "C" void kernel_launch(void* const* d_in, const int* in_sizes, int n_in, void* d_out, int out_size, void* d_ws,
                              size_t ws_size, hipStream_t stream) {
  Args a;
  for (int br = 0; br < 2; ++br) {
    a.x[br] = (const char*)d_in[5 * br + 0];
    a.k[br] = (const char*)d_in[5 * br + 1];
    a.v[br] = (const char*)d_in[5 * br + 2];
    a.wq[br] = (const char*)d_in[5 * br + 3];
    a.wo[br] = (const char*)d_in[5 * br + 4];
  }
  a.ws = (char*)d_ws;
  a.out = (unsigned short*)d_out;
  hipLaunchKernelGGL(attn_fused, dim3(NWG), dim3(NTHR), 0, stream, a);
}
```

```cpp
#include <hip/hip_runtime.h>
#include <hip/hip_bf16.h>
#include <stdint.h>

#define STAMPS 0
#define STAMPW 0
#define TAU_ 7060
#define TOFF_ 120
#define YD_ 25

namespace {

constexpr int DM = 640, NH = 32, SK = 4096, DH = 128, NB = 2;
constexpr int NSPLIT = 2;
constexpr int NWAVE = 8, NTHR = NWAVE * 64;
constexpr int KEYS_WG = SK / NSPLIT;
constexpr int NSTEP = KEYS_WG / (4 * NWAVE);
constexpr int STEP_B = NWAVE * 1024;
constexpr int RING = 16;
constexpr int NBODY = NSTEP / RING;
constexpr int FLIGHT = 4;
constexpr int NWG = 2 * NB * NH * NSPLIT;
constexpr int AUX_NT = 2, AUX_SC1 = 16;
constexpr unsigned SPIN_CAP = 1u << 16;
constexpr int OOB = 0x40000000;
constexpr int TAU = TAU_;
constexpr int T_OFF = TOFF_;
constexpr int DSHIFT = 6;
constexpr int Y_DELAY = YD_;

constexpr int WS_EPOCH = 0;
constexpr int WS_Q = 4096;
constexpr int WS_ML = WS_Q + 64 * 4 * 64 * 16;
constexpr int WS_O = WS_ML + 256 * 128;
constexpr int WS_Y = WS_O + 64 * 4 * 64 * 16;
constexpr int WS_END = WS_Y + 2 * 32 * 4 * 2 * 80 * 16;

typedef unsigned u32x4 __attribute__((ext_vector_type(4)));
typedef unsigned u32x3 __attribute__((ext_vector_type(3)));
typedef float f32x16 __attribute__((ext_vector_type(16)));
typedef float f32x4 __attribute__((ext_vector_type(4)));
typedef float f32x2 __attribute__((ext_vector_type(2)));
typedef __bf16 bf16x2 __attribute__((ext_vector_type(2)));
typedef __bf16 bf16x8 __attribute__((ext_vector_type(8)));

__device__ __forceinline__ float dot2(unsigned a, unsigned b, float acc) {
  return __builtin_amdgcn_fdot2_f32_bf16(__builtin_bit_cast(bf16x2, a), __builtin_bit_cast(bf16x2, b), acc, false);
}
__device__ __forceinline__ unsigned cvtpk(float lo, float hi) {
  bf16x2 r;
  r[0] = (__bf16)lo;
  r[1] = (__bf16)hi;
  return __builtin_bit_cast(unsigned, r);
}
template <int CTRL> __device__ __forceinline__ float dpp(float x) {
  return __builtin_bit_cast(float, __builtin_amdgcn_mov_dpp(__builtin_bit_cast(int, x), CTRL, 0xf, 0xf, true));
}
constexpr int XOR1 = 0xB1, XOR2 = 0x4E, XOR7 = 0x141, ROR8 = 0x128;
__device__ __forceinline__ unsigned perm_lo(unsigned a, unsigned b) { return __builtin_amdgcn_perm(b, a, 0x05040100u); }
__device__ __forceinline__ unsigned perm_hi(unsigned a, unsigned b) { return __builtin_amdgcn_perm(b, a, 0x07060302u); }
__device__ __forceinline__ float bf_lo(unsigned u) { return __uint_as_float(u << 16); }
__device__ __forceinline__ float bf_hi(unsigned u) { return __uint_as_float(u & 0xffff0000u); }

__device__ __forceinline__ float xrow_max(float x) {
  auto s = __builtin_amdgcn_permlane16_swap(__float_as_uint(x), __float_as_uint(x), false, false);
  x = fmaxf(__uint_as_float(s[0]), __uint_as_float(s[1]));
  auto t = __builtin_amdgcn_permlane32_swap(__float_as_uint(x), __float_as_uint(x), false, false);
  return fmaxf(__uint_as_float(t[0]), __uint_as_float(t[1]));
}
__device__ __forceinline__ float fold32(float a, float b) {
  auto t = __builtin_amdgcn_permlane32_swap(__float_as_uint(a), __float_as_uint(b), false, false);
  return __uint_as_float(t[0]) + __uint_as_float(t[1]);
}
__device__ __forceinline__ float fold16(float a, float b) {
  auto t = __builtin_amdgcn_permlane16_swap(__float_as_uint(a), __float_as_uint(b), false, false);
  return __uint_as_float(t[0]) + __uint_as_float(t[1]);
}
__device__ __forceinline__ float wave_sum(float x) {
  x += dpp<XOR1>(x); x += dpp<XOR2>(x); x += dpp<XOR7>(x); x += dpp<ROR8>(x);
  x = fold16(x, x);
  return fold32(x, x);
}
template <int N> __device__ __forceinline__ void wait_vm() {
  static_assert(N >= 0 && N < 64, "");
  __builtin_amdgcn_s_waitcnt((N & 15) | (7 << 4) | (15 << 8) | ((N >> 4) << 14));
}
__device__ __forceinline__ void wait_lvl(int lvl) {
  asm volatile(
      "s_waitcnt vmcnt(5)\n\t"
      "s_cmp_gt_i32 %0, 2\n\ts_cbranch_scc1 .Lw%=\n\ts_waitcnt vmcnt(4)\n\t"
      "s_cmp_gt_i32 %0, 1\n\ts_cbranch_scc1 .Lw%=\n\ts_waitcnt vmcnt(3)\n\t"
      "s_cmp_gt_i32 %0, 0\n\ts_cbranch_scc1 .Lw%=\n\ts_waitcnt vmcnt(2)\n"
      ".Lw%=:"
      :: "s"(lvl) : "scc", "memory");
}
__device__ __forceinline__ unsigned now_ticks() { return (unsigned)__builtin_amdgcn_s_memrealtime(); }

struct Args {
  const char* x[2];
  const char* k[2];
  const char* v[2];
  const char* wq[2];
  const char* wo[2];
  char* ws;
  unsigned short* out;
};

#define RSRC(p, n) __builtin_amdgcn_make_buffer_rsrc((void*)(p), 0, (n), 0x00020000)
#define BLD(r, vo, so, aux) __builtin_amdgcn_raw_buffer_load_b128((r), (vo), (so), (aux))
#define PUBLISH(v0, v1, off) __builtin_amdgcn_raw_buffer_store_b128(u32x4{__float_as_uint(v0), tagA, __float_as_uint(v1), tagB}, wsr, (off), 0, AUX_SC1)
#define TAG_OK(v) ((v)[1] == tagA && (v)[3] == tagB)
#define SB() __builtin_amdgcn_sched_barrier(0)
#define WAVE_FENCE() __builtin_amdgcn_fence(__ATOMIC_ACQ_REL, "wavefront")
#define STAMP(i)

constexpr int L_SC = 0;
constexpr int L_Y = 2048;
constexpr int L_ML = L_Y + 8 * 320;
constexpr int L_OB = L_ML + 16;
constexpr int L_CNT = L_OB + 8 * 16;
constexpr int L_TOT = 21 * 1024;
static_assert(L_CNT + 4 <= L_TOT, "");

__device__ __forceinline__ bool last_arriver(float* lds, int i) {
  unsigned old = 0;
  if ((threadIdx.x & 63) == 0) old = __hip_atomic_fetch_add(reinterpret_cast<unsigned*>(lds + L_CNT) + i, 1u, __ATOMIC_ACQ_REL, __HIP_MEMORY_SCOPE_WORKGROUP);
  return __builtin_amdgcn_readfirstlane(old) == NWAVE - 1;
}

__global__ void __launch_bounds__(NTHR, 2) attn_fused(Args a) {
  __shared__ __attribute__((aligned(16))) float lds[L_TOT];
  const int tid = threadIdx.x, lane = tid & 63, w = __builtin_amdgcn_readfirstlane(tid >> 6);
  const int g = lane >> 4, c = lane & 15;
  STAMP(0);
  const unsigned t_start = now_ticks();
  if (tid < 4) reinterpret_cast<unsigned*>(lds + L_CNT)[tid] = 0u;
  const char *px0 = a.x[0], *px1 = a.x[1], *pk0 = a.k[0], *pk1 = a.k[1], *pv0 = a.v[0], *pv1 = a.v[1], *pq0 = a.wq[0], *pq1 = a.wq[1], *po0 = a.wo[0], *po1 = a.wo[1];
  char* pws = a.ws;
  unsigned short* pout = a.out;
  asm volatile("" : "+s"(px0), "+s"(px1), "+s"(pk0), "+s"(pk1), "+s"(pv0), "+s"(pv1), "+s"(pq0), "+s"(pq1), "+s"(po0), "+s"(po1), "+s"(pws), "+s"(pout));
  const int bid = blockIdx.x, xcd = bid & 7, idx = bid >> 3;
  const int grp = xcd * 8 + (idx >> 2), mem = idx & 3;
  const int br = grp >> 5, h = grp & 31, b = mem >> 1, sp = mem & 1;
  const int wgi = grp * 4 + mem;
  const int rot = wgi & (NSTEP - 1);
  const auto wsr = RSRC(pws, WS_END);
  int q_so = WS_Q + grp * 4096, ml_so = WS_ML + (wgi & ~1) * 128, o_so = WS_O + grp * 4096, y_so = WS_Y + br * (32 * 4 * 2 * 80 * 16);
  asm volatile("" : "+s"(q_so), "+s"(ml_so), "+s"(o_so), "+s"(y_so));

  const unsigned epoch_v = __builtin_amdgcn_raw_buffer_load_b32(wsr, 0, WS_EPOCH + br * 256, AUX_SC1 | AUX_NT);
  const int row0 = 320 * b + 40 * w + 5 * (lane >> 3);
  u32x3 xr[2];
  {
    const auto xrs = RSRC(br ? px1 : px0, NB * DM * 2);
#pragma unroll
    for (int bb = 0; bb < 2; ++bb) xr[bb] = __builtin_amdgcn_raw_buffer_load_b96(xrs, (row0 * 2) & ~3, bb * DM * 2, AUX_NT);
  }
  const auto wqr = RSRC(br ? pq1 : pq0, DM * NH * DH * 2);
  u32x4 wq[5];
#pragma unroll
  for (int j = 0; j < 5; ++j) wq[j] = BLD(wqr, row0 * (NH * DH * 2) + 16 * (lane & 7), (h * DH + sp * 64) * 2 + j * (NH * DH * 2), AUX_NT);
  SB();
  __syncthreads();
  SB();
  const size_t kvoff = ((size_t)(b * NH + h) * SK + sp * KEYS_WG) * (DH * 2);
  const auto kr = RSRC((br ? pk1 : pk0) + kvoff, KEYS_WG * DH * 2);
  const auto vr = RSRC((br ? pv1 : pv0) + kvoff, KEYS_WG * DH * 2);
  const int kv_vo = w * 1024 + lane * 16;
#define SOFF(s) ((((s) + rot) & (NSTEP - 1)) * STEP_B)
  u32x4 ring[RING];
#pragma unroll
  for (int t = 0; t < FLIGHT; ++t) ring[t] = BLD(kr, kv_vo, SOFF(t), AUX_NT);
  SB();
  const unsigned epoch = __builtin_amdgcn_readfirstlane(epoch_v);
  const unsigned tagA = epoch + 1u, tagB = epoch ^ 0x9E3779B9u;

  {
    f32x2 qa[2][4];
#pragma unroll
    for (int bb = 0; bb < 2; ++bb) {
      const unsigned sh = (row0 & 1) * 16;
      const unsigned x01 = __builtin_amdgcn_alignbit(xr[bb][1], xr[bb][0], sh), x23 = __builtin_amdgcn_alignbit(xr[bb][2], xr[bb][1], sh), x4 = xr[bb][2] >> sh;
      const float xv[5] = {bf_lo(x01), bf_hi(x01), bf_lo(x23), bf_hi(x23), bf_lo(x4)};
#pragma unroll
      for (int d = 0; d < 4; ++d) {
        qa[bb][d] = f32x2{0.f, 0.f};
#pragma unroll
        for (int j = 0; j < 5; ++j) qa[bb][d] = __builtin_elementwise_fma(f32x2{bf_lo(wq[j][d]), bf_hi(wq[j][d])}, f32x2{xv[j], xv[j]}, qa[bb][d]);
      }
    }
    float f[8], e[4];
#pragma unroll
    for (int i = 0; i < 8; ++i) f[i] = fold32(qa[0][i >> 1][i & 1], qa[1][i >> 1][i & 1]);
#pragma unroll
    for (int i = 0; i < 4; ++i) { e[i] = fold16(f[i], f[i + 4]); e[i] += dpp<ROR8>(e[i]); }
    if (c < 8) *reinterpret_cast<f32x4*>(&lds[L_Y + w * 128 + (g >> 1) * 64 + 8 * c + 4 * (g & 1)]) = f32x4{e[0], e[1], e[2], e[3]};
  }
  if (last_arriver(lds, 0)) {
    f32x2 s = {0.f, 0.f};
#pragma unroll
    for (int ww = 0; ww < NWAVE; ++ww) s += *reinterpret_cast<const f32x2*>(&lds[L_Y + ww * 128 + 2 * lane]);
    PUBLISH(s[0], s[1], q_so + (((lane >> 5) * 2 + b) * 64 + sp * 32 + (lane & 31)) * 16);
  }
  STAMP(1);
  const auto wor = RSRC((br ? po1 : po0) + (size_t)h * DH * DM * 2, DH * DM * 2);
  const int wo_vo = (lane & 31) < 20 ? (16 * w + 8 * (lane >> 5)) * (DM * 2) + mem * 320 + 16 * (lane & 31) : OOB;
  u32x4 wo[8];
  u32x4 qg0, qg1;
  const int q_vo = (b * 128 + lane) * 16;
#pragma unroll
  for (int t = FLIGHT; t < RING; ++t) {
    wait_vm<FLIGHT - 1>();
    ring[t] = BLD(kr, kv_vo, SOFF(t), AUX_NT);
    SB();
  }
#pragma unroll
  for (int j = 0; j < 8; ++j) {
    if (j == 8 - FLIGHT) { qg0 = BLD(wsr, q_vo, q_so, AUX_SC1); qg1 = BLD(wsr, q_vo + 1024, q_so, AUX_SC1); }
    if (j >= 8 - FLIGHT) wait_vm<FLIGHT + 1>(); else wait_vm<FLIGHT - 1>();
    wo[j] = BLD(wor, wo_vo, j * (DM * 2), AUX_NT);
    SB();
  }
  if (!__all(TAG_OK(qg0) && TAG_OK(qg1))) {
#pragma unroll 1
    for (unsigned spins = 0; spins < SPIN_CAP; ++spins) {
      asm volatile("" ::: "memory");
      qg0 = BLD(wsr, q_vo, q_so, AUX_SC1); qg1 = BLD(wsr, q_vo + 1024, q_so, AUX_SC1);
      if (__all(TAG_OK(qg0) && TAG_OK(qg1))) break;
      __builtin_amdgcn_s_sleep(1);
    }
  }
  reinterpret_cast<unsigned*>(lds + L_SC)[w * 256 + lane] = cvtpk(__uint_as_float(qg0[0]) + __uint_as_float(qg1[0]), __uint_as_float(qg0[2]) + __uint_as_float(qg1[2]));
  WAVE_FENCE();
  const u32x4 qv = *reinterpret_cast<const u32x4*>(lds + L_SC + w * 256 + 4 * c);
  WAVE_FENCE();
  STAMP(2);

  unsigned t_ref = t_start + T_OFF + (((5 + 5 + RING) * TAU) >> 8);
  int lvl;
#define RELAG(T) { lvl = (int)(now_ticks() - t_ref - (unsigned)(((T) * TAU) >> 8)) >> DSHIFT; asm volatile("" : "+s"(lvl)); SB(); }

  float* scw = &lds[L_SC + (w * 4 + g) * NSTEP];
  float mx = -INFINITY;
#define KSTEP(T)                                                                        \
  {                                                                                     \
    float d = dot2(ring[T][0], qv[0], 0.f);                                             \
    d = dot2(ring[T][1], qv[1], d);                                                     \
    d = dot2(ring[T][2], qv[2], d);                                                     \
    d = dot2(ring[T][3], qv[3], d);                                                     \
    x[(T) & 3] = d;                                                                     \
    asm volatile("" : "+v"(x[(T) & 3]));                                                \
    SB();                                                                               \
    wait_lvl(lvl);                                                                      \
    ring[T] = BLD(nr, kv_vo, SOFF(RING * it + RING + (T)), AUX_NT);                     \
    SB();                                                                               \
    if (((T) & 3) == 3) {     \
      const float a0 = x[0] + dpp<ROR8>(x[0]), a1 = x[1] + dpp<ROR8>(x[1]), a2 = x[2] + dpp<ROR8>(x[2]), a3 = x[3] + dpp<ROR8>(x[3]); \
      const float b0 = (c & 8) ? a2 : a0, b1 = (c & 8) ? a3 : a1;                       \
      const float c0 = b0 + dpp<XOR7>(b0), c1 = b1 + dpp<XOR7>(b1);                     \
      float s = (c & 4) ? c1 : c0;                                                      \
      s += dpp<XOR2>(s);                                                                \
      s += dpp<XOR1>(s);                                                                \
      mx = fmaxf(mx, s);                                                                \
      scw[RING * it + (T) - 3 + (c >> 2)] = s;                                          \
      SB();                                                                             \
      if ((T) != 15) RELAG((T) + 1)                                                     \
    }                                                                                   \
  }
#pragma unroll 1
  for (int it = 0; it < NBODY; ++it) {
    const auto nr = (it == NBODY - 1) ? vr : kr;
    float x[4];
    RELAG(0)
    KSTEP(0) KSTEP(1) KSTEP(2) KSTEP(3) KSTEP(4) KSTEP(5) KSTEP(6) KSTEP(7)
    KSTEP(8) KSTEP(9) KSTEP(10) KSTEP(11) KSTEP(12) KSTEP(13) KSTEP(14) KSTEP(15)
    t_ref += (RING * TAU) >> 8;
  }
#undef KSTEP
  STAMP(3);
  constexpr float C2 = 0.088388347648318440f * 1.4426950408889634f;
  mx = fmaxf(mx, dpp<ROR8>(mx));
  mx = fmaxf(mx, dpp<XOR7>(mx));
  mx = xrow_max(mx) * C2;
  WAVE_FENCE();
  {
    f32x4 s4 = *reinterpret_cast<const f32x4*>(scw + 4 * c);
#pragma unroll
    for (int t = 0; t < 4; ++t) s4[t] = __builtin_amdgcn_exp2f(fmaf(s4[t], C2, -mx));
    *reinterpret_cast<f32x4*>(scw + 4 * c) = s4;
    const float ls = wave_sum((s4[0] + s4[1]) + (s4[2] + s4[3]));
    if (lane == 0) *reinterpret_cast<f32x2*>(&lds[L_ML + 2 * w]) = f32x2{mx, ls};
  }
  if (last_arriver(lds, 1)) {
    f32x4 ml[4];
#pragma unroll
    for (int i = 0; i < 4; ++i) ml[i] = *reinterpret_cast<const f32x4*>(&lds[L_ML + 4 * i]);
    const float mwg = fmaxf(fmaxf(fmaxf(ml[0][0], ml[0][2]), fmaxf(ml[1][0], ml[1][2])), fmaxf(fmaxf(ml[2][0], ml[2][2]), fmaxf(ml[3][0], ml[3][2])));
    float lwg = 0.f;
#pragma unroll
    for (int i = 0; i < 4; ++i) lwg += ml[i][1] * __builtin_amdgcn_exp2f(ml[i][0] - mwg) + ml[i][3] * __builtin_amdgcn_exp2f(ml[i][2] - mwg);
    if (lane == 0) PUBLISH(mwg, lwg, ml_so + sp * 128);
  }
  u32x4 wb[8];
#pragma unroll
  for (int i = 0; i < 8; ++i)
#pragma unroll
    for (int jj = 0; jj < 4; ++jj) wb[i][jj] = (i & 1) ? perm_hi(wo[2 * jj][i >> 1], wo[2 * jj + 1][i >> 1]) : perm_lo(wo[2 * jj][i >> 1], wo[2 * jj + 1][i >> 1]);
#pragma unroll
  for (int i = 0; i < 8; ++i) asm volatile("" : "+v"(wb[i]));
  SB();

  f32x2 acc2[4];
#pragma unroll
  for (int i = 0; i < 4; ++i) acc2[i] = f32x2{0.f, 0.f};
  u32x4 mlp;
  const int ml_vo = (lane & 1) * 128;
#define VSTEP(T, LOAD)                                                                  \
  {                                                                                     \
    if (((T) & 3) == 0) p4 = *reinterpret_cast<const f32x4*>(scw + RING * it + (T));    \
    _Pragma("unroll") for (int d = 0; d < 4; ++d)                                       \
      acc2[d] = __builtin_elementwise_fma(f32x2{bf_lo(ring[T][d]), bf_hi(ring[T][d])}, f32x2{p4[(T) & 3], p4[(T) & 3]}, acc2[d]); \
    asm volatile("" : "+v"(acc2[0]), "+v"(acc2[1]), "+v"(acc2[2]), "+v"(acc2[3]));     \
    SB();                                                                               \
    if (LOAD) { wait_lvl(lvl); ring[T] = BLD(vr, kv_vo, SOFF(RING * it + RING + (T)), AUX_NT); } \
    SB();                                                                               \
    if (LOAD && ((T) & 3) == 3 && (T) != 15) RELAG((T) + 1)                             \
  }
#define VBODY(LOAD) { f32x4 p4;                                                         \
    VSTEP(0, LOAD) VSTEP(1, LOAD) VSTEP(2, LOAD) VSTEP(3, LOAD) VSTEP(4, LOAD) VSTEP(5, LOAD) VSTEP(6, LOAD) VSTEP(7, LOAD) \
    VSTEP(8, LOAD) VSTEP(9, LOAD) VSTEP(10, LOAD) VSTEP(11, LOAD) VSTEP(12, LOAD) VSTEP(13, LOAD) VSTEP(14, LOAD) VSTEP(15, LOAD) }
#pragma unroll 1
  for (int it = 0; it < NBODY - 1; ++it) {
    mlp = BLD(wsr, it == NBODY - 2 ? ml_vo : OOB, ml_so, AUX_SC1);
    SB();
    RELAG(0)
    VBODY(true)
    t_ref += (RING * TAU) >> 8;
  }
  if (!__all(TAG_OK(mlp))) {
#pragma unroll 1
    for (unsigned spins = 0; spins < SPIN_CAP; ++spins) {
      asm volatile("" ::: "memory");
      mlp = BLD(wsr, ml_vo, ml_so, AUX_SC1);
      if (__all(TAG_OK(mlp))) break;
      __builtin_amdgcn_s_sleep(1);
    }
  }
  float scale;
  {
    const float m1 = __uint_as_float(mlp[0]), l1 = __uint_as_float(mlp[2]);
    const float M = fmaxf(m1, dpp<XOR1>(m1));
    const float t = l1 * __builtin_amdgcn_exp2f(m1 - M);
    scale = __builtin_amdgcn_exp2f(mx - M) / (t + dpp<XOR1>(t));
  }
  asm volatile("" : "+v"(scale));
  SB();
  { const int it = NBODY - 1; VBODY(false) }
#undef VBODY
#undef VSTEP
  WAVE_FENCE();
  STAMP(4);
  {
    const float f0 = fold32(acc2[0][0], acc2[2][0]), f1 = fold32(acc2[0][1], acc2[2][1]), f2 = fold32(acc2[1][0], acc2[3][0]), f3 = fold32(acc2[1][1], acc2[3][1]);
    const float e0 = fold16(f0, f2), e1 = fold16(f1, f3);
    *reinterpret_cast<f32x2*>(&lds[L_SC + w * 256 + 8 * c + 2 * (g & 1) + 4 * (g >> 1)]) = f32x2{e0 * scale, e1 * scale};
    typedef __attribute__((address_space(3))) float lds_f;
    const unsigned cnt_a = (unsigned)(size_t)(lds_f*)&lds[L_CNT + 2], rd_a = (unsigned)(size_t)(lds_f*)&lds[L_SC + 2 * lane];
    unsigned old;
    unsigned long long sv;
    f32x4 s0, s1, s2, s3;
    asm volatile(
        "s_mov_b64 %[sv], exec\n\ts_mov_b64 exec, 1\n\t"
        "ds_add_rtn_u32 %[old], %[ca], %[one]\n\t"
        "s_mov_b64 exec, %[sv]\n\t"
        "ds_read2st64_b64 %[s0], %[ra] offset1:2\n\t"
        "ds_read2st64_b64 %[s1], %[ra] offset0:4 offset1:6\n\t"
        "ds_read2st64_b64 %[s2], %[ra] offset0:8 offset1:10\n\t"
        "ds_read2st64_b64 %[s3], %[ra] offset0:12 offset1:14\n\t"
        "s_waitcnt lgkmcnt(0)"
        : [sv] "=&s"(sv), [old] "=&v"(old), [s0] "=&v"(s0), [s1] "=&v"(s1), [s2] "=&v"(s2), [s3] "=&v"(s3)
        : [ca] "v"(cnt_a), [one] "v"(1u), [ra] "v"(rd_a)
        : "memory");
    if (__builtin_amdgcn_readfirstlane(old) == NWAVE - 1) {
      const f32x4 u = (s0 + s1) + (s2 + s3);
      PUBLISH(u[0] + u[2], u[1] + u[3], o_so + mem * 1024 + lane * 16);
    }
  }
  {
    const int o_vo = ((lane >> 3) & 3) * 1024 + (8 * w + (lane & 7)) * 16;
    u32x4 og;
#pragma unroll 1
    for (unsigned spins = 0; spins < SPIN_CAP; ++spins) {
      asm volatile("" ::: "memory");
      og = BLD(wsr, o_vo, o_so, AUX_SC1);
      if (__all(TAG_OK(og))) break;
    }
    float o0 = __uint_as_float(og[0]), o1 = __uint_as_float(og[2]);
    o0 += dpp<ROR8>(o0);
    o1 += dpp<ROR8>(o1);
    reinterpret_cast<unsigned*>(lds + L_OB)[w * 16 + (g & 1) * 8 + (lane & 7)] = cvtpk(o0, o1);
  }
  WAVE_FENCE();
  STAMP(5);
  {
    const u32x4 oa = *reinterpret_cast<const u32x4*>(lds + L_OB + w * 16 + ((lane >> 2) & 1) * 8 + 4 * (lane >> 5));
    float y[8];
    const f32x16 z = {0.f, 0.f, 0.f, 0.f, 0.f, 0.f, 0.f, 0.f, 0.f, 0.f, 0.f, 0.f, 0.f, 0.f, 0.f, 0.f};
#pragma unroll
    for (int i = 0; i < 8; ++i)
      y[i] = __builtin_amdgcn_mfma_f32_32x32x16_bf16(__builtin_bit_cast(bf16x8, oa), __builtin_bit_cast(bf16x8, wb[i]), z, 0, 0, 0)[0];
    if ((lane & 31) < 20) {
      float* yp = &lds[L_Y + w * 320 + (lane >> 5) * 160 + 8 * (lane & 31)];
      *reinterpret_cast<f32x4*>(yp) = f32x4{y[0], y[1], y[2], y[3]};
      *reinterpret_cast<f32x4*>(yp + 4) = f32x4{y[4], y[5], y[6], y[7]};
    }
  }
  __syncthreads();
  if (tid < 160) {
    f32x2 s[8];
#pragma unroll
    for (int ww = 0; ww < 8; ++ww) s[ww] = *reinterpret_cast<const f32x2*>(&lds[L_Y + ww * 320 + 2 * tid]);
    const f32x2 t = ((s[0] + s[1]) + (s[2] + s[3])) + ((s[4] + s[5]) + (s[6] + s[7]));
    PUBLISH(t[0], t[1], y_so + (h * 4 + mem) * 2560 + tid * 16);
  }
  STAMP(6);
  const int unit = wgi & 127;
  if (w == 3 && unit < 80) {
    const int y_vo = (lane >> 3) * (4 * 2560) + unit * 128 + (lane & 7) * 16;
    __builtin_amdgcn_s_sleep(Y_DELAY);
    u32x4 yg[4];
#pragma unroll 1
    for (unsigned spins = 0; spins < SPIN_CAP; ++spins) {
      asm volatile("" ::: "memory");
#pragma unroll
      for (int i = 0; i < 4; ++i) yg[i] = BLD(wsr, y_vo + i * (8 * 4 * 2560), y_so, AUX_SC1);
      unsigned bad = 0;
#pragma unroll
      for (int i = 0; i < 4; ++i) bad |= (yg[i][1] ^ tagA) | (yg[i][3] ^ tagB);
      if (__all(bad == 0)) break;
    }
    const float s0 = (__uint_as_float(yg[0][0]) + __uint_as_float(yg[1][0])) + (__uint_as_float(yg[2][0]) + __uint_as_float(yg[3][0]));
    const float s1 = (__uint_as_float(yg[0][2]) + __uint_as_float(yg[1][2])) + (__uint_as_float(yg[2][2]) + __uint_as_float(yg[3][2]));
    float s = fold32(s0, s1);
    s = fold16(s, s);
    s += dpp<ROR8>(s);
    auto t = __builtin_amdgcn_permlane32_swap(__float_as_uint(s), __float_as_uint(s), false, false);
    const int col = (unit / 20) * 160 + (unit % 10) * 16 + 2 * lane;
    if (lane < 8) *reinterpret_cast<unsigned*>(pout + ((unit / 10) & 1) * (2 * DM) + br * DM + col) = cvtpk(__uint_as_float(t[0]), __uint_as_float(t[1]));
    if (unit == 0 && lane == 0) __builtin_amdgcn_raw_buffer_store_b32(epoch + 1u, wsr, 0, WS_EPOCH + br * 256, AUX_SC1);
  }
}

}

extern "C" void kernel_launch(void* const* d_in, const int* in_sizes, int n_in, void* d_out, int out_size, void* d_ws,
                              size_t ws_size, hipStream_t stream) {
  Args a;
  for (int br = 0; br < 2; ++br) {
    a.x[br] = (const char*)d_in[5 * br + 0];
    a.k[br] = (const char*)d_in[5 * br + 1];
    a.v[br] = (const char*)d_in[5 * br + 2];
    a.wq[br] = (const char*)d_in[5 * br + 3];
    a.wo[br] = (const char*)d_in[5 * br + 4];
  }
  a.ws = (char*)d_ws;
  a.out = (unsigned short*)d_out;
  hipLaunchKernelGGL(attn_fused, dim3(NWG), dim3(NTHR), 0, stream, a);
}
```

```cpp
#include <hip/hip_runtime.h>
#include <hip/hip_bf16.h>
#include <stdint.h>

namespace {

constexpr int DM = 640, NH = 32, SK = 4096, DH = 128, NB = 2;
constexpr int NSPLIT = 2;
constexpr int NWAVE = 8, NTHR = NWAVE * 64;
constexpr int KEYS_WG = SK / NSPLIT;
constexpr int NSTEP = KEYS_WG / (4 * NWAVE);
constexpr int STEP_B = NWAVE * 1024;
constexpr int RING = 16;
constexpr int NBODY = NSTEP / RING;
constexpr int FLIGHT = 4;
constexpr int NWG = 2 * NB * NH * NSPLIT;
constexpr int AUX_NT = 2, AUX_SC1 = 16;
constexpr unsigned SPIN_CAP = 1u << 16;
constexpr int OOB = 0x40000000;
constexpr int TAU = 6950;
constexpr int T_OFF = 140;
constexpr int DSHIFT = 6;
constexpr int Y_DELAY = 25;

constexpr int WS_EPOCH = 0;
constexpr int WS_Q = 4096;
constexpr int WS_ML = WS_Q + 64 * 4 * 64 * 16;
constexpr int WS_O = WS_ML + 256 * 128;
constexpr int WS_Y = WS_O + 64 * 4 * 64 * 16;
constexpr int WS_END = WS_Y + 2 * 32 * 4 * 2 * 80 * 16;

typedef unsigned u32x4 __attribute__((ext_vector_type(4)));
typedef unsigned u32x3 __attribute__((ext_vector_type(3)));
typedef float f32x16 __attribute__((ext_vector_type(16)));
typedef float f32x4 __attribute__((ext_vector_type(4)));
typedef float f32x2 __attribute__((ext_vector_type(2)));
typedef __bf16 bf16x2 __attribute__((ext_vector_type(2)));
typedef __bf16 bf16x8 __attribute__((ext_vector_type(8)));

__device__ __forceinline__ float dot2(unsigned a, unsigned b, float acc) {
  return __builtin_amdgcn_fdot2_f32_bf16(__builtin_bit_cast(bf16x2, a), __builtin_bit_cast(bf16x2, b), acc, false);
}
__device__ __forceinline__ unsigned cvtpk(float lo, float hi) {
  bf16x2 r;
  r[0] = (__bf16)lo;
  r[1] = (__bf16)hi;
  return __builtin_bit_cast(unsigned, r);
}
template <int CTRL> __device__ __forceinline__ float dpp(float x) {
  return __builtin_bit_cast(float, __builtin_amdgcn_mov_dpp(__builtin_bit_cast(int, x), CTRL, 0xf, 0xf, true));
}
constexpr int XOR1 = 0xB1, XOR2 = 0x4E, XOR7 = 0x141, ROR8 = 0x128;
__device__ __forceinline__ unsigned perm_lo(unsigned a, unsigned b) { return __builtin_amdgcn_perm(b, a, 0x05040100u); }
__device__ __forceinline__ unsigned perm_hi(unsigned a, unsigned b) { return __builtin_amdgcn_perm(b, a, 0x07060302u); }
__device__ __forceinline__ float bf_lo(unsigned u) { return __uint_as_float(u << 16); }
__device__ __forceinline__ float bf_hi(unsigned u) { return __uint_as_float(u & 0xffff0000u); }

__device__ __forceinline__ float xrow_max(float x) {
  auto s = __builtin_amdgcn_permlane16_swap(__float_as_uint(x), __float_as_uint(x), false, false);
  x = fmaxf(__uint_as_float(s[0]), __uint_as_float(s[1]));
  auto t = __builtin_amdgcn_permlane32_swap(__float_as_uint(x), __float_as_uint(x), false, false);
  return fmaxf(__uint_as_float(t[0]), __uint_as_float(t[1]));
}
__device__ __forceinline__ float fold32(float a, float b) {
  auto t = __builtin_amdgcn_permlane32_swap(__float_as_uint(a), __float_as_uint(b), false, false);
  return __uint_as_float(t[0]) + __uint_as_float(t[1]);
}
__device__ __forceinline__ float fold16(float a, float b) {
  auto t = __builtin_amdgcn_permlane16_swap(__float_as_uint(a), __float_as_uint(b), false, false);
  return __uint_as_float(t[0]) + __uint_as_float(t[1]);
}
__device__ __forceinline__ float wave_sum(float x) {
  x += dpp<XOR1>(x); x += dpp<XOR2>(x); x += dpp<XOR7>(x); x += dpp<ROR8>(x);
  x = fold16(x, x);
  return fold32(x, x);
}
template <int N> __device__ __forceinline__ void wait_vm() {
  static_assert(N >= 0 && N < 64, "");
  __builtin_amdgcn_s_waitcnt((N & 15) | (7 << 4) | (15 << 8) | ((N >> 4) << 14));
}
__device__ __forceinline__ void wait_lvl(int lvl) {
  asm volatile(
      "s_waitcnt vmcnt(5)\n\t"
      "s_cmp_gt_i32 %0, 2\n\ts_cbranch_scc1 .Lw%=\n\ts_waitcnt vmcnt(4)\n\t"
      "s_cmp_gt_i32 %0, 1\n\ts_cbranch_scc1 .Lw%=\n\ts_waitcnt vmcnt(3)\n\t"
      "s_cmp_gt_i32 %0, 0\n\ts_cbranch_scc1 .Lw%=\n\ts_waitcnt vmcnt(2)\n"
      ".Lw%=:"
      :: "s"(lvl) : "scc", "memory");
}
__device__ __forceinline__ unsigned now_ticks() { return (unsigned)__builtin_amdgcn_s_memrealtime(); }

struct Args {
  const char* x[2];
  const char* k[2];
  const char* v[2];
  const char* wq[2];
  const char* wo[2];
  char* ws;
  unsigned short* out;
};

#define RSRC(p, n) __builtin_amdgcn_make_buffer_rsrc((void*)(p), 0, (n), 0x00020000)
#define BLD(r, vo, so, aux) __builtin_amdgcn_raw_buffer_load_b128((r), (vo), (so), (aux))
#define PUBLISH(v0, v1, off) __builtin_amdgcn_raw_buffer_store_b128(u32x4{__float_as_uint(v0), tagA, __float_as_uint(v1), tagB}, wsr, (off), 0, AUX_SC1)
#define TAG_OK(v) ((v)[1] == tagA && (v)[3] == tagB)
#define SB() __builtin_amdgcn_sched_barrier(0)
#define WAVE_FENCE() __builtin_amdgcn_fence(__ATOMIC_ACQ_REL, "wavefront")

constexpr int L_SC = 0;
constexpr int L_Y = 2048;
constexpr int L_ML = L_Y + 8 * 320;
constexpr int L_OB = L_ML + 16;
constexpr int L_CNT = L_OB + 8 * 16;
constexpr int L_TOT = 21 * 1024;
static_assert(L_CNT + 4 <= L_TOT, "");

__device__ __forceinline__ bool last_arriver(float* lds, int i) {
  unsigned old = 0;
  if ((threadIdx.x & 63) == 0) old = __hip_atomic_fetch_add(reinterpret_cast<unsigned*>(lds + L_CNT) + i, 1u, __ATOMIC_ACQ_REL, __HIP_MEMORY_SCOPE_WORKGROUP);
  return __builtin_amdgcn_readfirstlane(old) == NWAVE - 1;
}

__global__ void __launch_bounds__(NTHR, 2) attn_fused(Args a) {
  __shared__ __attribute__((aligned(16))) float lds[L_TOT];
  const int tid = threadIdx.x, lane = tid & 63, w = __builtin_amdgcn_readfirstlane(tid >> 6);
  const int g = lane >> 4, c = lane & 15;
  const unsigned t_start = now_ticks();
  if (tid < 4) reinterpret_cast<unsigned*>(lds + L_CNT)[tid] = 0u;
  const char *px0 = a.x[0], *px1 = a.x[1], *pk0 = a.k[0], *pk1 = a.k[1], *pv0 = a.v[0], *pv1 = a.v[1], *pq0 = a.wq[0], *pq1 = a.wq[1], *po0 = a.wo[0], *po1 = a.wo[1];
  char* pws = a.ws;
  unsigned short* pout = a.out;
  asm volatile("" : "+s"(px0), "+s"(px1), "+s"(pk0), "+s"(pk1), "+s"(pv0), "+s"(pv1), "+s"(pq0), "+s"(pq1), "+s"(po0), "+s"(po1), "+s"(pws), "+s"(pout));
  const int bid = blockIdx.x, xcd = bid & 7, idx = bid >> 3;
  const int grp = xcd * 8 + (idx >> 2), mem = idx & 3;
  const int br = grp >> 5, h = grp & 31, b = mem >> 1, sp = mem & 1;
  const int wgi = grp * 4 + mem;
  const int rot = wgi & (NSTEP - 1);
  const auto wsr = RSRC(pws, WS_END);
  int q_so = WS_Q + grp * 4096, ml_so = WS_ML + (wgi & ~1) * 128, o_so = WS_O + grp * 4096, y_so = WS_Y + br * (32 * 4 * 2 * 80 * 16);
  asm volatile("" : "+s"(q_so), "+s"(ml_so), "+s"(o_so), "+s"(y_so));

  const u32x4 epoch_v = BLD(wsr, 0, WS_EPOCH + br * 256, AUX_SC1 | AUX_NT);
  const int row0 = 320 * b + 40 * w + 5 * (lane >> 3);
  u32x3 xr[2];
  {
    const auto xrs = RSRC(br ? px1 : px0, NB * DM * 2);
#pragma unroll
    for (int bb = 0; bb < 2; ++bb) xr[bb] = __builtin_amdgcn_raw_buffer_load_b96(xrs, (row0 * 2) & ~3, bb * DM * 2, AUX_NT);
  }
  const auto wqr = RSRC(br ? pq1 : pq0, DM * NH * DH * 2);
  u32x4 wq[5];
#pragma unroll
  for (int j = 0; j < 5; ++j) wq[j] = BLD(wqr, row0 * (NH * DH * 2) + 16 * (lane & 7), (h * DH + sp * 64) * 2 + j * (NH * DH * 2), AUX_NT);
  SB();
  __syncthreads();
  SB();
  const size_t kvoff = ((size_t)(b * NH + h) * SK + sp * KEYS_WG) * (DH * 2);
  const auto kr = RSRC((br ? pk1 : pk0) + kvoff, KEYS_WG * DH * 2);
  const auto vr = RSRC((br ? pv1 : pv0) + kvoff, KEYS_WG * DH * 2);
  const int kv_vo = w * 1024 + lane * 16;
#define SOFF(s) ((((s) + rot) & (NSTEP - 1)) * STEP_B)
  u32x4 ring[RING];
#pragma unroll
  for (int t = 0; t < FLIGHT; ++t) ring[t] = BLD(kr, kv_vo, SOFF(t), AUX_NT);
  SB();
  const unsigned e0 = __builtin_amdgcn_readfirstlane(epoch_v[0]), e1 = __builtin_amdgcn_readfirstlane(epoch_v[1]),
                 e2 = __builtin_amdgcn_readfirstlane(epoch_v[2]), e3 = __builtin_amdgcn_readfirstlane(epoch_v[3]);
  const unsigned epoch = e0 + (e1 - e0) * 0x10001u + (e2 - e0) * 0x101u + (e3 - e0) * 0x1001u;
  const unsigned tagA = epoch + 1u, tagB = epoch ^ 0x9E3779B9u;

  {
    f32x2 qa[2][4];
#pragma unroll
    for (int bb = 0; bb < 2; ++bb) {
      const unsigned sh = (row0 & 1) * 16;
      const unsigned x01 = __builtin_amdgcn_alignbit(xr[bb][1], xr[bb][0], sh), x23 = __builtin_amdgcn_alignbit(xr[bb][2], xr[bb][1], sh), x4 = xr[bb][2] >> sh;
      const float xv[5] = {bf_lo(x01), bf_hi(x01), bf_lo(x23), bf_hi(x23), bf_lo(x4)};
#pragma unroll
      for (int d = 0; d < 4; ++d) {
        qa[bb][d] = f32x2{0.f, 0.f};
#pragma unroll
        for (int j = 0; j < 5; ++j) qa[bb][d] = __builtin_elementwise_fma(f32x2{bf_lo(wq[j][d]), bf_hi(wq[j][d])}, f32x2{xv[j], xv[j]}, qa[bb][d]);
      }
    }
    float f[8], e[4];
#pragma unroll
    for (int i = 0; i < 8; ++i) f[i] = fold32(qa[0][i >> 1][i & 1], qa[1][i >> 1][i & 1]);
#pragma unroll
    for (int i = 0; i < 4; ++i) { e[i] = fold16(f[i], f[i + 4]); e[i] += dpp<ROR8>(e[i]); }
    if (c < 8) *reinterpret_cast<f32x4*>(&lds[L_Y + w * 128 + (g >> 1) * 64 + 8 * c + 4 * (g & 1)]) = f32x4{e[0], e[1], e[2], e[3]};
  }
  if (last_arriver(lds, 0)) {
    f32x2 s = {0.f, 0.f};
#pragma unroll
    for (int ww = 0; ww < NWAVE; ++ww) s += *reinterpret_cast<const f32x2*>(&lds[L_Y + ww * 128 + 2 * lane]);
    PUBLISH(s[0], s[1], q_so + (((lane >> 5) * 2 + b) * 64 + sp * 32 + (lane & 31)) * 16);
  }
  const auto wor = RSRC((br ? po1 : po0) + (size_t)h * DH * DM * 2, DH * DM * 2);
  const int wo_vo = (lane & 31) < 20 ? (16 * w + 8 * (lane >> 5)) * (DM * 2) + mem * 320 + 16 * (lane & 31) : OOB;
  u32x4 wo[8];
  u32x4 qg0, qg1;
  const int q_vo = (b * 128 + lane) * 16;
#pragma unroll
  for (int t = FLIGHT; t < RING; ++t) {
    wait_vm<FLIGHT - 1>();
    ring[t] = BLD(kr, kv_vo, SOFF(t), AUX_NT);
    SB();
  }
#pragma unroll
  for (int j = 0; j < 8; ++j) {
    if (j == 8 - FLIGHT) { qg0 = BLD(wsr, q_vo, q_so, AUX_SC1); qg1 = BLD(wsr, q_vo + 1024, q_so, AUX_SC1); }
    if (j >= 8 - FLIGHT) wait_vm<FLIGHT + 1>(); else wait_vm<FLIGHT - 1>();
    wo[j] = BLD(wor, wo_vo, j * (DM * 2), AUX_NT);
    SB();
  }
  if (!__all(TAG_OK(qg0) && TAG_OK(qg1))) {
#pragma unroll 1
    for (unsigned spins = 0; spins < SPIN_CAP; ++spins) {
      asm volatile("" ::: "memory");
      qg0 = BLD(wsr, q_vo, q_so, AUX_SC1); qg1 = BLD(wsr, q_vo + 1024, q_so, AUX_SC1);
      if (__all(TAG_OK(qg0) && TAG_OK(qg1))) break;
      __builtin_amdgcn_s_sleep(1);
    }
  }
  reinterpret_cast<unsigned*>(lds + L_SC)[w * 256 + lane] = cvtpk(__uint_as_float(qg0[0]) + __uint_as_float(qg1[0]), __uint_as_float(qg0[2]) + __uint_as_float(qg1[2]));
  WAVE_FENCE();
  const u32x4 qv = *reinterpret_cast<const u32x4*>(lds + L_SC + w * 256 + 4 * c);
  WAVE_FENCE();
  __builtin_amdgcn_s_barrier();

  unsigned t_ref = t_start + T_OFF + (((5 + 5 + RING) * TAU) >> 8);
  int lvl;
#define RELAG(T) { lvl = (int)(now_ticks() - t_ref - (unsigned)(((T) * TAU) >> 8)) >> DSHIFT; asm volatile("" : "+s"(lvl)); SB(); }

  float* scw = &lds[L_SC + (w * 4 + g) * NSTEP];
  float mx = -INFINITY;
#define KSTEP(T)                                                                        \
  {                                                                                     \
    float d = dot2(ring[T][0], qv[0], 0.f);                                             \
    d = dot2(ring[T][1], qv[1], d);                                                     \
    d = dot2(ring[T][2], qv[2], d);                                                     \
    d = dot2(ring[T][3], qv[3], d);                                                     \
    x[(T) & 3] = d;                                                                     \
    asm volatile("" : "+v"(x[(T) & 3]));                                                \
    SB();                                                                               \
    wait_lvl(lvl);                                                                      \
    ring[T] = BLD(nr, kv_vo, SOFF(RING * it + RING + (T)), AUX_NT);                     \
    SB();                                                                               \
    if (((T) & 3) == 3) {     \
      const float a0 = x[0] + dpp<ROR8>(x[0]), a1 = x[1] + dpp<ROR8>(x[1]), a2 = x[2] + dpp<ROR8>(x[2]), a3 = x[3] + dpp<ROR8>(x[3]); \
      const float b0 = (c & 8) ? a2 : a0, b1 = (c & 8) ? a3 : a1;                       \
      const float c0 = b0 + dpp<XOR7>(b0), c1 = b1 + dpp<XOR7>(b1);                     \
      float s = (c & 4) ? c1 : c0;                                                      \
      s += dpp<XOR2>(s);                                                                \
      s += dpp<XOR1>(s);                                                                \
      mx = fmaxf(mx, s);                                                                \
      scw[RING * it + (T) - 3 + (c >> 2)] = s;                                          \
      SB();                                                                             \
      if ((T) != 15) RELAG((T) + 1)                                                     \
    }                                                                                   \
  }
#pragma unroll 1
  for (int it = 0; it < NBODY; ++it) {
    const auto nr = (it == NBODY - 1) ? vr : kr;
    float x[4];
    RELAG(0)
    KSTEP(0) KSTEP(1) KSTEP(2) KSTEP(3) KSTEP(4) KSTEP(5) KSTEP(6) KSTEP(7)
    KSTEP(8) KSTEP(9) KSTEP(10) KSTEP(11) KSTEP(12) KSTEP(13) KSTEP(14) KSTEP(15)
    t_ref += (RING * TAU) >> 8;
  }
#undef KSTEP
  constexpr float C2 = 0.088388347648318440f * 1.4426950408889634f;
  mx = fmaxf(mx, dpp<ROR8>(mx));
  mx = fmaxf(mx, dpp<XOR7>(mx));
  mx = xrow_max(mx) * C2;
  WAVE_FENCE();
  {
    f32x4 s4 = *reinterpret_cast<const f32x4*>(scw + 4 * c);
#pragma unroll
    for (int t = 0; t < 4; ++t) s4[t] = __builtin_amdgcn_exp2f(fmaf(s4[t], C2, -mx));
    *reinterpret_cast<f32x4*>(scw + 4 * c) = s4;
    const float ls = wave_sum((s4[0] + s4[1]) + (s4[2] + s4[3]));
    if (lane == 0) *reinterpret_cast<f32x2*>(&lds[L_ML + 2 * w]) = f32x2{mx, ls};
  }
  if (last_arriver(lds, 1)) {
    f32x4 ml[4];
#pragma unroll
    for (int i = 0; i < 4; ++i) ml[i] = *reinterpret_cast<const f32x4*>(&lds[L_ML + 4 * i]);
    const float mwg = fmaxf(fmaxf(fmaxf(ml[0][0], ml[0][2]), fmaxf(ml[1][0], ml[1][2])), fmaxf(fmaxf(ml[2][0], ml[2][2]), fmaxf(ml[3][0], ml[3][2])));
    float lwg = 0.f;
#pragma unroll
    for (int i = 0; i < 4; ++i) lwg += ml[i][1] * __builtin_amdgcn_exp2f(ml[i][0] - mwg) + ml[i][3] * __builtin_amdgcn_exp2f(ml[i][2] - mwg);
    if (lane == 0) PUBLISH(mwg, lwg, ml_so + sp * 128);
  }
  u32x4 wb[8];
#pragma unroll
  for (int i = 0; i < 8; ++i)
#pragma unroll
    for (int jj = 0; jj < 4; ++jj) wb[i][jj] = (i & 1) ? perm_hi(wo[2 * jj][i >> 1], wo[2 * jj + 1][i >> 1]) : perm_lo(wo[2 * jj][i >> 1], wo[2 * jj + 1][i >> 1]);
#pragma unroll
  for (int i = 0; i < 8; ++i) asm volatile("" : "+v"(wb[i]));
  SB();

  f32x2 acc2[4];
#pragma unroll
  for (int i = 0; i < 4; ++i) acc2[i] = f32x2{0.f, 0.f};
  u32x4 mlp;
  const int ml_vo = (lane & 1) * 128;
#define VSTEP(T, LOAD)                                                                  \
  {                                                                                     \
    if (((T) & 3) == 0) p4 = *reinterpret_cast<const f32x4*>(scw + RING * it + (T));    \
    _Pragma("unroll") for (int d = 0; d < 4; ++d)                                       \
      acc2[d] = __builtin_elementwise_fma(f32x2{bf_lo(ring[T][d]), bf_hi(ring[T][d])}, f32x2{p4[(T) & 3], p4[(T) & 3]}, acc2[d]); \
    asm volatile("" : "+v"(acc2[0]), "+v"(acc2[1]), "+v"(acc2[2]), "+v"(acc2[3]));     \
    SB();                                                                               \
    if (LOAD) { wait_lvl(lvl); ring[T] = BLD(vr, kv_vo, SOFF(RING * it + RING + (T)), AUX_NT); } \
    SB();                                                                               \
    if (LOAD && ((T) & 3) == 3 && (T) != 15) RELAG((T) + 1)                             \
  }
#define VBODY(LOAD) { f32x4 p4;                                                         \
    VSTEP(0, LOAD) VSTEP(1, LOAD) VSTEP(2, LOAD) VSTEP(3, LOAD) VSTEP(4, LOAD) VSTEP(5, LOAD) VSTEP(6, LOAD) VSTEP(7, LOAD) \
    VSTEP(8, LOAD) VSTEP(9, LOAD) VSTEP(10, LOAD) VSTEP(11, LOAD) VSTEP(12, LOAD) VSTEP(13, LOAD) VSTEP(14, LOAD) VSTEP(15, LOAD) }
#pragma unroll 1
  for (int it = 0; it < NBODY - 1; ++it) {
    mlp = BLD(wsr, it == NBODY - 2 ? ml_vo : OOB, ml_so, AUX_SC1);
    SB();
    RELAG(0)
    VBODY(true)
    t_ref += (RING * TAU) >> 8;
  }
  if (!__all(TAG_OK(mlp))) {
#pragma unroll 1
    for (unsigned spins = 0; spins < SPIN_CAP; ++spins) {
      asm volatile("" ::: "memory");
      mlp = BLD(wsr, ml_vo, ml_so, AUX_SC1);
      if (__all(TAG_OK(mlp))) break;
      __builtin_amdgcn_s_sleep(1);
    }
  }
  float scale;
  {
    const float m1 = __uint_as_float(mlp[0]), l1 = __uint_as_float(mlp[2]);
    const float M = fmaxf(m1, dpp<XOR1>(m1));
    const float t = l1 * __builtin_amdgcn_exp2f(m1 - M);
    scale = __builtin_amdgcn_exp2f(mx - M) / (t + dpp<XOR1>(t));
  }
  asm volatile("" : "+v"(scale));
  SB();
  { const int it = NBODY - 1; VBODY(false) }
#undef VBODY
#undef VSTEP
  WAVE_FENCE();
  {
    const float f0 = fold32(acc2[0][0], acc2[2][0]), f1 = fold32(acc2[0][1], acc2[2][1]), f2 = fold32(acc2[1][0], acc2[3][0]), f3 = fold32(acc2[1][1], acc2[3][1]);
    const float r0 = fold16(f0, f2), r1 = fold16(f1, f3);
    *reinterpret_cast<f32x2*>(&lds[L_SC + w * 256 + 8 * c + 2 * (g & 1) + 4 * (g >> 1)]) = f32x2{r0 * scale, r1 * scale};
    typedef __attribute__((address_space(3))) float lds_f;
    const unsigned cnt_a = (unsigned)(size_t)(lds_f*)&lds[L_CNT + 2], rd_a = (unsigned)(size_t)(lds_f*)&lds[L_SC + 2 * lane];
    unsigned old;
    unsigned long long sv;
    f32x4 s0, s1, s2, s3;
    asm volatile(
        "s_mov_b64 %[sv], exec\n\ts_mov_b64 exec, 1\n\t"
        "ds_add_rtn_u32 %[old], %[ca], %[one]\n\t"
        "s_mov_b64 exec, %[sv]\n\t"
        "ds_read2st64_b64 %[s0], %[ra] offset1:2\n\t"
        "ds_read2st64_b64 %[s1], %[ra] offset0:4 offset1:6\n\t"
        "ds_read2st64_b64 %[s2], %[ra] offset0:8 offset1:10\n\t"
        "ds_read2st64_b64 %[s3], %[ra] offset0:12 offset1:14\n\t"
        "s_waitcnt lgkmcnt(0)"
        : [sv] "=&s"(sv), [old] "=&v"(old), [s0] "=&v"(s0), [s1] "=&v"(s1), [s2] "=&v"(s2), [s3] "=&v"(s3)
        : [ca] "v"(cnt_a), [one] "v"(1u), [ra] "v"(rd_a)
        : "memory");
    if (__builtin_amdgcn_readfirstlane(old) == NWAVE - 1) {
      const f32x4 u = (s0 + s1) + (s2 + s3);
      PUBLISH(u[0] + u[2], u[1] + u[3], o_so + mem * 1024 + lane * 16);
    }
  }
  {
    const int o_vo = ((lane >> 3) & 3) * 1024 + (8 * w + (lane & 7)) * 16;
    u32x4 og;
#pragma unroll 1
    for (unsigned spins = 0; spins < SPIN_CAP; ++spins) {
      asm volatile("" ::: "memory");
      og = BLD(wsr, o_vo, o_so, AUX_SC1);
      if (__all(TAG_OK(og))) break;
    }
    float o0 = __uint_as_float(og[0]), o1 = __uint_as_float(og[2]);
    o0 += dpp<ROR8>(o0);
    o1 += dpp<ROR8>(o1);
    reinterpret_cast<unsigned*>(lds + L_OB)[w * 16 + (g & 1) * 8 + (lane & 7)] = cvtpk(o0, o1);
  }
  WAVE_FENCE();
  {
    const u32x4 oa = *reinterpret_cast<const u32x4*>(lds + L_OB + w * 16 + ((lane >> 2) & 1) * 8 + 4 * (lane >> 5));
    float y[8];
    const f32x16 z = {0.f, 0.f, 0.f, 0.f, 0.f, 0.f, 0.f, 0.f, 0.f, 0.f, 0.f, 0.f, 0.f, 0.f, 0.f, 0.f};
#pragma unroll
    for (int i = 0; i < 8; ++i)
      y[i] = __builtin_amdgcn_mfma_f32_32x32x16_bf16(__builtin_bit_cast(bf16x8, oa), __builtin_bit_cast(bf16x8, wb[i]), z, 0, 0, 0)[0];
    if ((lane & 31) < 20) {
      float* yp = &lds[L_Y + w * 320 + (lane >> 5) * 160 + 8 * (lane & 31)];
      *reinterpret_cast<f32x4*>(yp) = f32x4{y[0], y[1], y[2], y[3]};
      *reinterpret_cast<f32x4*>(yp + 4) = f32x4{y[4], y[5], y[6], y[7]};
    }
  }
  __syncthreads();
  if (tid < 160) {
    f32x2 s[8];
#pragma unroll
    for (int ww = 0; ww < 8; ++ww) s[ww] = *reinterpret_cast<const f32x2*>(&lds[L_Y + ww * 320 + 2 * tid]);
    const f32x2 t = ((s[0] + s[1]) + (s[2] + s[3])) + ((s[4] + s[5]) + (s[6] + s[7]));
    PUBLISH(t[0], t[1], y_so + (h * 4 + mem) * 2560 + tid * 16);
  }
  const int unit = wgi & 127;
  if (w == 3 && unit < 80) {
    const int y_vo = (lane >> 3) * (4 * 2560) + unit * 128 + (lane & 7) * 16;
    __builtin_amdgcn_s_sleep(Y_DELAY);
    u32x4 yg[4];
#pragma unroll 1
    for (unsigned spins = 0; spins < SPIN_CAP; ++spins) {
      asm volatile("" ::: "memory");
#pragma unroll
      for (int i = 0; i < 4; ++i) yg[i] = BLD(wsr, y_vo + i * (8 * 4 * 2560), y_so, AUX_SC1);
      unsigned bad = 0;
#pragma unroll
      for (int i = 0; i < 4; ++i) bad |= (yg[i][1] ^ tagA) | (yg[i][3] ^ tagB);
      if (__all(bad == 0)) break;
    }
    const float s0 = (__uint_as_float(yg[0][0]) + __uint_as_float(yg[1][0])) + (__uint_as_float(yg[2][0]) + __uint_as_float(yg[3][0]));
    const float s1 = (__uint_as_float(yg[0][2]) + __uint_as_float(yg[1][2])) + (__uint_as_float(yg[2][2]) + __uint_as_float(yg[3][2]));
    float s = fold32(s0, s1);
    s = fold16(s, s);
    s += dpp<ROR8>(s);
    auto t = __builtin_amdgcn_permlane32_swap(__float_as_uint(s), __float_as_uint(s), false, false);
    const int col = (unit / 20) * 160 + (unit % 10) * 16 + 2 * lane;
    if (lane < 8) *reinterpret_cast<unsigned*>(pout + ((unit / 10) & 1) * (2 * DM) + br * DM + col) = cvtpk(__uint_as_float(t[0]), __uint_as_float(t[1]));
    if (unit == 0 && lane == 0) __builtin_amdgcn_raw_buffer_store_b128(u32x4{e0 + 1u, e1 + 1u, e2 + 1u, e3 + 1u}, wsr, WS_EPOCH + br * 256, 0, AUX_SC1);
  }
}

}

extern "C" void kernel_launch(void* const* d_in, const int* in_sizes, int n_in, void* d_out, int out_size, void* d_ws,
                              size_t ws_size, hipStream_t stream) {
  Args a;
  for (int br = 0; br < 2; ++br) {
    a.x[br] = (const char*)d_in[5 * br + 0];
    a.k[br] = (const char*)d_in[5 * br + 1];
    a.v[br] = (const char*)d_in[5 * br + 2];
    a.wq[br] = (const char*)d_in[5 * br + 3];
    a.wo[br] = (const char*)d_in[5 * br + 4];
  }
  a.ws = (char*)d_ws;
  a.out = (unsigned short*)d_out;
  hipLaunchKernelGGL(attn_fused, dim3(NWG), dim3(NTHR), 0, stream, a);
}
```

```cpp
#include <hip/hip_runtime.h>
#include <hip/hip_bf16.h>
#include <stdint.h>

#define STAMPS 0
#define STAMPW 0
#define ALIGN 1
#define TAU_ 6900
#define TOFF_ 190
#define YD_ 25

namespace {

constexpr int DM = 640, NH = 32, SK = 4096, DH = 128, NB = 2;
constexpr int NSPLIT = 2;
constexpr int NWAVE = 8, NTHR = NWAVE * 64;
constexpr int KEYS_WG = SK / NSPLIT;
constexpr int NSTEP = KEYS_WG / (4 * NWAVE);
constexpr int STEP_B = NWAVE * 1024;
constexpr int RING = 16;
constexpr int NBODY = NSTEP / RING;
constexpr int FLIGHT = 4;
constexpr int NWG = 2 * NB * NH * NSPLIT;
constexpr int AUX_NT = 2, AUX_SC1 = 16;
constexpr unsigned SPIN_CAP = 1u << 16;
constexpr int OOB = 0x40000000;
constexpr int TAU = TAU_;
constexpr int T_OFF = TOFF_;
constexpr int DSHIFT = 6;
constexpr int Y_DELAY = YD_;

constexpr int WS_EPOCH = 0;
constexpr int WS_Q = 4096;
constexpr int WS_ML = WS_Q + 64 * 4 * 64 * 16;
constexpr int WS_O = WS_ML + 256 * 128;
constexpr int WS_Y = WS_O + 64 * 4 * 64 * 16;
constexpr int WS_END = WS_Y + 2 * 32 * 4 * 2 * 80 * 16;

typedef unsigned u32x4 __attribute__((ext_vector_type(4)));
typedef unsigned u32x3 __attribute__((ext_vector_type(3)));
typedef float f32x16 __attribute__((ext_vector_type(16)));
typedef float f32x4 __attribute__((ext_vector_type(4)));
typedef float f32x2 __attribute__((ext_vector_type(2)));
typedef __bf16 bf16x2 __attribute__((ext_vector_type(2)));
typedef __bf16 bf16x8 __attribute__((ext_vector_type(8)));

__device__ __forceinline__ float dot2(unsigned a, unsigned b, float acc) {
  return __builtin_amdgcn_fdot2_f32_bf16(__builtin_bit_cast(bf16x2, a), __builtin_bit_cast(bf16x2, b), acc, false);
}
__device__ __forceinline__ unsigned cvtpk(float lo, float hi) {
  bf16x2 r;
  r[0] = (__bf16)lo;
  r[1] = (__bf16)hi;
  return __builtin_bit_cast(unsigned, r);
}
template <int CTRL> __device__ __forceinline__ float dpp(float x) {
  return __builtin_bit_cast(float, __builtin_amdgcn_mov_dpp(__builtin_bit_cast(int, x), CTRL, 0xf, 0xf, true));
}
constexpr int XOR1 = 0xB1, XOR2 = 0x4E, XOR7 = 0x141, ROR8 = 0x128;
__device__ __forceinline__ unsigned perm_lo(unsigned a, unsigned b) { return __builtin_amdgcn_perm(b, a, 0x05040100u); }
__device__ __forceinline__ unsigned perm_hi(unsigned a, unsigned b) { return __builtin_amdgcn_perm(b, a, 0x07060302u); }
__device__ __forceinline__ float bf_lo(unsigned u) { return __uint_as_float(u << 16); }
__device__ __forceinline__ float bf_hi(unsigned u) { return __uint_as_float(u & 0xffff0000u); }

__device__ __forceinline__ float xrow_max(float x) {
  auto s = __builtin_amdgcn_permlane16_swap(__float_as_uint(x), __float_as_uint(x), false, false);
  x = fmaxf(__uint_as_float(s[0]), __uint_as_float(s[1]));
  auto t = __builtin_amdgcn_permlane32_swap(__float_as_uint(x), __float_as_uint(x), false, false);
  return fmaxf(__uint_as_float(t[0]), __uint_as_float(t[1]));
}
__device__ __forceinline__ float fold32(float a, float b) {
  auto t = __builtin_amdgcn_permlane32_swap(__float_as_uint(a), __float_as_uint(b), false, false);
  return __uint_as_float(t[0]) + __uint_as_float(t[1]);
}
__device__ __forceinline__ float fold16(float a, float b) {
  auto t = __builtin_amdgcn_permlane16_swap(__float_as_uint(a), __float_as_uint(b), false, false);
  return __uint_as_float(t[0]) + __uint_as_float(t[1]);
}
__device__ __forceinline__ float wave_sum(float x) {
  x += dpp<XOR1>(x); x += dpp<XOR2>(x); x += dpp<XOR7>(x); x += dpp<ROR8>(x);
  x = fold16(x, x);
  return fold32(x, x);
}
template <int N> __device__ __forceinline__ void wait_vm() {
  static_assert(N >= 0 && N < 64, "");
  __builtin_amdgcn_s_waitcnt((N & 15) | (7 << 4) | (15 << 8) | ((N >> 4) << 14));
}
__device__ __forceinline__ void wait_lvl(int lvl) {
  asm volatile(
      "s_waitcnt vmcnt(5)\n\t"
      "s_cmp_gt_i32 %0, 2\n\ts_cbranch_scc1 .Lw%=\n\ts_waitcnt vmcnt(4)\n\t"
      "s_cmp_gt_i32 %0, 1\n\ts_cbranch_scc1 .Lw%=\n\ts_waitcnt vmcnt(3)\n\t"
      "s_cmp_gt_i32 %0, 0\n\ts_cbranch_scc1 .Lw%=\n\ts_waitcnt vmcnt(2)\n"
      ".Lw%=:"
      :: "s"(lvl) : "scc", "memory");
}
__device__ __forceinline__ unsigned now_ticks() { return (unsigned)__builtin_amdgcn_s_memrealtime(); }

struct Args {
  const char* x[2];
  const char* k[2];
  const char* v[2];
  const char* wq[2];
  const char* wo[2];
  char* ws;
  unsigned short* out;
};

#define RSRC(p, n) __builtin_amdgcn_make_buffer_rsrc((void*)(p), 0, (n), 0x00020000)
#define BLD(r, vo, so, aux) __builtin_amdgcn_raw_buffer_load_b128((r), (vo), (so), (aux))
#define PUBLISH(v0, v1, off) __builtin_amdgcn_raw_buffer_store_b128(u32x4{__float_as_uint(v0), tagA, __float_as_uint(v1), tagB}, wsr, (off), 0, AUX_SC1)
#define TAG_OK(v) ((v)[1] == tagA && (v)[3] == tagB)
#define SB() __builtin_amdgcn_sched_barrier(0)
#define WAVE_FENCE() __builtin_amdgcn_fence(__ATOMIC_ACQ_REL, "wavefront")
#define STAMP(i)

constexpr int L_SC = 0;
constexpr int L_Y = 2048;
constexpr int L_ML = L_Y + 8 * 320;
constexpr int L_OB = L_ML + 16;
constexpr int L_CNT = L_OB + 8 * 16;
constexpr int L_TOT = 21 * 1024;
static_assert(L_CNT + 4 <= L_TOT, "");

__device__ __forceinline__ bool last_arriver(float* lds, int i) {
  unsigned old = 0;
  if ((threadIdx.x & 63) == 0) old = __hip_atomic_fetch_add(reinterpret_cast<unsigned*>(lds + L_CNT) + i, 1u, __ATOMIC_ACQ_REL, __HIP_MEMORY_SCOPE_WORKGROUP);
  return __builtin_amdgcn_readfirstlane(old) == NWAVE - 1;
}

__global__ void __launch_bounds__(NTHR, 2) attn_fused(Args a) {
  __shared__ __attribute__((aligned(16))) float lds[L_TOT];
  const int tid = threadIdx.x, lane = tid & 63, w = __builtin_amdgcn_readfirstlane(tid >> 6);
  const int g = lane >> 4, c = lane & 15;
  STAMP(0);
  const unsigned t_start = now_ticks();
  if (tid < 4) reinterpret_cast<unsigned*>(lds + L_CNT)[tid] = 0u;
  const char *px0 = a.x[0], *px1 = a.x[1], *pk0 = a.k[0], *pk1 = a.k[1], *pv0 = a.v[0], *pv1 = a.v[1], *pq0 = a.wq[0], *pq1 = a.wq[1], *po0 = a.wo[0], *po1 = a.wo[1];
  char* pws = a.ws;
  unsigned short* pout = a.out;
  asm volatile("" : "+s"(px0), "+s"(px1), "+s"(pk0), "+s"(pk1), "+s"(pv0), "+s"(pv1), "+s"(pq0), "+s"(pq1), "+s"(po0), "+s"(po1), "+s"(pws), "+s"(pout));
  const int bid = blockIdx.x, xcd = bid & 7, idx = bid >> 3;
  const int grp = xcd * 8 + (idx >> 2), mem = idx & 3;
  const int br = grp >> 5, h = grp & 31, b = mem >> 1, sp = mem & 1;
  const int wgi = grp * 4 + mem;
  const int rot = wgi & (NSTEP - 1);
  const auto wsr = RSRC(pws, WS_END);
  int q_so = WS_Q + grp * 4096, ml_so = WS_ML + (wgi & ~1) * 128, o_so = WS_O + grp * 4096, y_so = WS_Y + br * (32 * 4 * 2 * 80 * 16);
  asm volatile("" : "+s"(q_so), "+s"(ml_so), "+s"(o_so), "+s"(y_so));

  const u32x4 epoch_v = BLD(wsr, 0, WS_EPOCH + br * 256, AUX_SC1 | AUX_NT);
  const int row0 = 320 * b + 40 * w + 5 * (lane >> 3);
  u32x3 xr[2];
  {
    const auto xrs = RSRC(br ? px1 : px0, NB * DM * 2);
#pragma unroll
    for (int bb = 0; bb < 2; ++bb) xr[bb] = __builtin_amdgcn_raw_buffer_load_b96(xrs, (row0 * 2) & ~3, bb * DM * 2, AUX_NT);
  }
  const auto wqr = RSRC(br ? pq1 : pq0, DM * NH * DH * 2);
  u32x4 wq[5];
#pragma unroll
  for (int j = 0; j < 5; ++j) wq[j] = BLD(wqr, row0 * (NH * DH * 2) + 16 * (lane & 7), (h * DH + sp * 64) * 2 + j * (NH * DH * 2), AUX_NT);
  SB();
  __syncthreads();
  SB();
  const size_t kvoff = ((size_t)(b * NH + h) * SK + sp * KEYS_WG) * (DH * 2);
  const auto kr = RSRC((br ? pk1 : pk0) + kvoff, KEYS_WG * DH * 2);
  const auto vr = RSRC((br ? pv1 : pv0) + kvoff, KEYS_WG * DH * 2);
  const int kv_vo = w * 1024 + lane * 16;
#define SOFF(s) ((((s) + rot) & (NSTEP - 1)) * STEP_B)
  u32x4 ring[RING];
#pragma unroll
  for (int t = 0; t < FLIGHT; ++t) ring[t] = BLD(kr, kv_vo, SOFF(t), AUX_NT);
  SB();
  const unsigned e0 = __builtin_amdgcn_readfirstlane(epoch_v[0]), e1 = __builtin_amdgcn_readfirstlane(epoch_v[1]),
                 e2 = __builtin_amdgcn_readfirstlane(epoch_v[2]), e3 = __builtin_amdgcn_readfirstlane(epoch_v[3]);
  const unsigned epoch = e0 + (e1 - e0) * 0x10001u + (e2 - e0) * 0x101u + (e3 - e0) * 0x1001u;
  const unsigned tagA = epoch + 1u, tagB = epoch ^ 0x9E3779B9u;

  {
    f32x2 qa[2][4];
#pragma unroll
    for (int bb = 0; bb < 2; ++bb) {
      const unsigned sh = (row0 & 1) * 16;
      const unsigned x01 = __builtin_amdgcn_alignbit(xr[bb][1], xr[bb][0], sh), x23 = __builtin_amdgcn_alignbit(xr[bb][2], xr[bb][1], sh), x4 = xr[bb][2] >> sh;
      const float xv[5] = {bf_lo(x01), bf_hi(x01), bf_lo(x23), bf_hi(x23), bf_lo(x4)};
#pragma unroll
      for (int d = 0; d < 4; ++d) {
        qa[bb][d] = f32x2{0.f, 0.f};
#pragma unroll
        for (int j = 0; j < 5; ++j) qa[bb][d] = __builtin_elementwise_fma(f32x2{bf_lo(wq[j][d]), bf_hi(wq[j][d])}, f32x2{xv[j], xv[j]}, qa[bb][d]);
      }
    }
    float f[8], e[4];
#pragma unroll
    for (int i = 0; i < 8; ++i) f[i] = fold32(qa[0][i >> 1][i & 1], qa[1][i >> 1][i & 1]);
#pragma unroll
    for (int i = 0; i < 4; ++i) { e[i] = fold16(f[i], f[i + 4]); e[i] += dpp<ROR8>(e[i]); }
    if (c < 8) *reinterpret_cast<f32x4*>(&lds[L_Y + w * 128 + (g >> 1) * 64 + 8 * c + 4 * (g & 1)]) = f32x4{e[0], e[1], e[2], e[3]};
  }
  if (last_arriver(lds, 0)) {
    f32x2 s = {0.f, 0.f};
#pragma unroll
    for (int ww = 0; ww < NWAVE; ++ww) s += *reinterpret_cast<const f32x2*>(&lds[L_Y + ww * 128 + 2 * lane]);
    PUBLISH(s[0], s[1], q_so + (((lane >> 5) * 2 + b) * 64 + sp * 32 + (lane & 31)) * 16);
  }
  STAMP(1);
  const auto wor = RSRC((br ? po1 : po0) + (size_t)h * DH * DM * 2, DH * DM * 2);
  const int wo_vo = (lane & 31) < 20 ? (16 * w + 8 * (lane >> 5)) * (DM * 2) + mem * 320 + 16 * (lane & 31) : OOB;
  u32x4 wo[8];
  u32x4 qg0, qg1;
  const int q_vo = (b * 128 + lane) * 16;
#pragma unroll
  for (int t = FLIGHT; t < RING; ++t) {
    wait_vm<FLIGHT - 1>();
    ring[t] = BLD(kr, kv_vo, SOFF(t), AUX_NT);
    SB();
  }
#pragma unroll
  for (int j = 0; j < 8; ++j) {
    if (j == 8 - FLIGHT) { qg0 = BLD(wsr, q_vo, q_so, AUX_SC1); qg1 = BLD(wsr, q_vo + 1024, q_so, AUX_SC1); }
    if (j >= 8 - FLIGHT) wait_vm<FLIGHT + 1>(); else wait_vm<FLIGHT - 1>();
    wo[j] = BLD(wor, wo_vo, j * (DM * 2), AUX_NT);
    SB();
  }
  if (!__all(TAG_OK(qg0) && TAG_OK(qg1))) {
#pragma unroll 1
    for (unsigned spins = 0; spins < SPIN_CAP; ++spins) {
      asm volatile("" ::: "memory");
      qg0 = BLD(wsr, q_vo, q_so, AUX_SC1); qg1 = BLD(wsr, q_vo + 1024, q_so, AUX_SC1);
      if (__all(TAG_OK(qg0) && TAG_OK(qg1))) break;
      __builtin_amdgcn_s_sleep(1);
    }
  }
  reinterpret_cast<unsigned*>(lds + L_SC)[w * 256 + lane] = cvtpk(__uint_as_float(qg0[0]) + __uint_as_float(qg1[0]), __uint_as_float(qg0[2]) + __uint_as_float(qg1[2]));
  WAVE_FENCE();
  const u32x4 qv = *reinterpret_cast<const u32x4*>(lds + L_SC + w * 256 + 4 * c);
  WAVE_FENCE();
  STAMP(2);

  unsigned t_ref = t_start + T_OFF + (((5 + 5 + RING) * TAU) >> 8);
  int lvl;
#define RELAG(T) { if (ALIGN == 4 || (ALIGN == 5 && ((T) & 7) == 0)) __builtin_amdgcn_s_barrier(); lvl = (int)(now_ticks() - t_ref - (unsigned)(((T) * TAU) >> 8)) >> DSHIFT; asm volatile("" : "+s"(lvl)); SB(); }

  float* scw = &lds[L_SC + (w * 4 + g) * NSTEP];
  float mx = -INFINITY;
#define KSTEP(T)                                                                        \
  {                                                                                     \
    float d = dot2(ring[T][0], qv[0], 0.f);                                             \
    d = dot2(ring[T][1], qv[1], d);                                                     \
    d = dot2(ring[T][2], qv[2], d);                                                     \
    d = dot2(ring[T][3], qv[3], d);                                                     \
    x[(T) & 3] = d;                                                                     \
    asm volatile("" : "+v"(x[(T) & 3]));                                                \
    SB();                                                                               \
    wait_lvl(lvl);                                                                      \
    ring[T] = BLD(nr, kv_vo, SOFF(RING * it + RING + (T)), AUX_NT);                     \
    SB();                                                                               \
    if (((T) & 3) == 3) {     \
      const float a0 = x[0] + dpp<ROR8>(x[0]), a1 = x[1] + dpp<ROR8>(x[1]), a2 = x[2] + dpp<ROR8>(x[2]), a3 = x[3] + dpp<ROR8>(x[3]); \
      const float b0 = (c & 8) ? a2 : a0, b1 = (c & 8) ? a3 : a1;                       \
      const float c0 = b0 + dpp<XOR7>(b0), c1 = b1 + dpp<XOR7>(b1);                     \
      float s = (c & 4) ? c1 : c0;                                                      \
      s += dpp<XOR2>(s);                                                                \
      s += dpp<XOR1>(s);                                                                \
      mx = fmaxf(mx, s);                                                                \
      scw[RING * it + (T) - 3 + (c >> 2)] = s;                                          \
      SB();                                                                             \
      if ((T) != 15) RELAG((T) + 1)                                                     \
    }                                                                                   \
  }
#pragma unroll 1
  for (int it = 0; it < NBODY; ++it) {
    const auto nr = (it == NBODY - 1) ? vr : kr;
    float x[4];
    if (ALIGN == 2 || (ALIGN == 1 && it == 0) || (ALIGN == 3 && it == 1)) __builtin_amdgcn_s_barrier();
    RELAG(0)
    KSTEP(0) KSTEP(1) KSTEP(2) KSTEP(3) KSTEP(4) KSTEP(5) KSTEP(6) KSTEP(7)
    KSTEP(8) KSTEP(9) KSTEP(10) KSTEP(11) KSTEP(12) KSTEP(13) KSTEP(14) KSTEP(15)
    t_ref += (RING * TAU) >> 8;
  }
#undef KSTEP
  STAMP(3);
  constexpr float C2 = 0.088388347648318440f * 1.4426950408889634f;
  mx = fmaxf(mx, dpp<ROR8>(mx));
  mx = fmaxf(mx, dpp<XOR7>(mx));
  mx = xrow_max(mx) * C2;
  WAVE_FENCE();
  {
    f32x4 s4 = *reinterpret_cast<const f32x4*>(scw + 4 * c);
#pragma unroll
    for (int t = 0; t < 4; ++t) s4[t] = __builtin_amdgcn_exp2f(fmaf(s4[t], C2, -mx));
    *reinterpret_cast<f32x4*>(scw + 4 * c) = s4;
    const float ls = wave_sum((s4[0] + s4[1]) + (s4[2] + s4[3]));
    if (lane == 0) *reinterpret_cast<f32x2*>(&lds[L_ML + 2 * w]) = f32x2{mx, ls};
  }
  if (last_arriver(lds, 1)) {
    f32x4 ml[4];
#pragma unroll
    for (int i = 0; i < 4; ++i) ml[i] = *reinterpret_cast<const f32x4*>(&lds[L_ML + 4 * i]);
    const float mwg = fmaxf(fmaxf(fmaxf(ml[0][0], ml[0][2]), fmaxf(ml[1][0], ml[1][2])), fmaxf(fmaxf(ml[2][0], ml[2][2]), fmaxf(ml[3][0], ml[3][2])));
    float lwg = 0.f;
#pragma unroll
    for (int i = 0; i < 4; ++i) lwg += ml[i][1] * __builtin_amdgcn_exp2f(ml[i][0] - mwg) + ml[i][3] * __builtin_amdgcn_exp2f(ml[i][2] - mwg);
    if (lane == 0) PUBLISH(mwg, lwg, ml_so + sp * 128);
  }
  u32x4 wb[8];
#pragma unroll
  for (int i = 0; i < 8; ++i)
#pragma unroll
    for (int jj = 0; jj < 4; ++jj) wb[i][jj] = (i & 1) ? perm_hi(wo[2 * jj][i >> 1], wo[2 * jj + 1][i >> 1]) : perm_lo(wo[2 * jj][i >> 1], wo[2 * jj + 1][i >> 1]);
#pragma unroll
  for (int i = 0; i < 8; ++i) asm volatile("" : "+v"(wb[i]));
  SB();

  f32x2 acc2[4];
#pragma unroll
  for (int i = 0; i < 4; ++i) acc2[i] = f32x2{0.f, 0.f};
  u32x4 mlp;
  const int ml_vo = (lane & 1) * 128;
#define VSTEP(T, LOAD)                                                                  \
  {                                                                                     \
    if (((T) & 3) == 0) p4 = *reinterpret_cast<const f32x4*>(scw + RING * it + (T));    \
    _Pragma("unroll") for (int d = 0; d < 4; ++d)                                       \
      acc2[d] = __builtin_elementwise_fma(f32x2{bf_lo(ring[T][d]), bf_hi(ring[T][d])}, f32x2{p4[(T) & 3], p4[(T) & 3]}, acc2[d]); \
    asm volatile("" : "+v"(acc2[0]), "+v"(acc2[1]), "+v"(acc2[2]), "+v"(acc2[3]));     \
    SB();                                                                               \
    if (LOAD) { wait_lvl(lvl); ring[T] = BLD(vr, kv_vo, SOFF(RING * it + RING + (T)), AUX_NT); } \
    SB();                                                                               \
    if (LOAD && ((T) & 3) == 3 && (T) != 15) RELAG((T) + 1)                             \
  }
#define VBODY(LOAD) { f32x4 p4;                                                         \
    VSTEP(0, LOAD) VSTEP(1, LOAD) VSTEP(2, LOAD) VSTEP(3, LOAD) VSTEP(4, LOAD) VSTEP(5, LOAD) VSTEP(6, LOAD) VSTEP(7, LOAD) \
    VSTEP(8, LOAD) VSTEP(9, LOAD) VSTEP(10, LOAD) VSTEP(11, LOAD) VSTEP(12, LOAD) VSTEP(13, LOAD) VSTEP(14, LOAD) VSTEP(15, LOAD) }
#pragma unroll 1
  for (int it = 0; it < NBODY - 1; ++it) {
    mlp = BLD(wsr, it == NBODY - 2 ? ml_vo : OOB, ml_so, AUX_SC1);
    SB();
    if (ALIGN == 2) __builtin_amdgcn_s_barrier();
    RELAG(0)
    VBODY(true)
    t_ref += (RING * TAU) >> 8;
  }
  if (!__all(TAG_OK(mlp))) {
#pragma unroll 1
    for (unsigned spins = 0; spins < SPIN_CAP; ++spins) {
      asm volatile("" ::: "memory");
      mlp = BLD(wsr, ml_vo, ml_so, AUX_SC1);
      if (__all(TAG_OK(mlp))) break;
      __builtin_amdgcn_s_sleep(1);
    }
  }
  float scale;
  {
    const float m1 = __uint_as_float(mlp[0]), l1 = __uint_as_float(mlp[2]);
    const float M = fmaxf(m1, dpp<XOR1>(m1));
    const float t = l1 * __builtin_amdgcn_exp2f(m1 - M);
    scale = __builtin_amdgcn_exp2f(mx - M) / (t + dpp<XOR1>(t));
  }
  asm volatile("" : "+v"(scale));
  SB();
  { const int it = NBODY - 1; VBODY(false) }
#undef VBODY
#undef VSTEP
  WAVE_FENCE();
  STAMP(4);
  {
    const float f0 = fold32(acc2[0][0], acc2[2][0]), f1 = fold32(acc2[0][1], acc2[2][1]), f2 = fold32(acc2[1][0], acc2[3][0]), f3 = fold32(acc2[1][1], acc2[3][1]);
    const float r0 = fold16(f0, f2), r1 = fold16(f1, f3);
    *reinterpret_cast<f32x2*>(&lds[L_SC + w * 256 + 8 * c + 2 * (g & 1) + 4 * (g >> 1)]) = f32x2{r0 * scale, r1 * scale};
    typedef __attribute__((address_space(3))) float lds_f;
    const unsigned cnt_a = (unsigned)(size_t)(lds_f*)&lds[L_CNT + 2], rd_a = (unsigned)(size_t)(lds_f*)&lds[L_SC + 2 * lane];
    unsigned old;
    unsigned long long sv;
    f32x4 s0, s1, s2, s3;
    asm volatile(
        "s_mov_b64 %[sv], exec\n\ts_mov_b64 exec, 1\n\t"
        "ds_add_rtn_u32 %[old], %[ca], %[one]\n\t"
        "s_mov_b64 exec, %[sv]\n\t"
        "ds_read2st64_b64 %[s0], %[ra] offset1:2\n\t"
        "ds_read2st64_b64 %[s1], %[ra] offset0:4 offset1:6\n\t"
        "ds_read2st64_b64 %[s2], %[ra] offset0:8 offset1:10\n\t"
        "ds_read2st64_b64 %[s3], %[ra] offset0:12 offset1:14\n\t"
        "s_waitcnt lgkmcnt(0)"
        : [sv] "=&s"(sv), [old] "=&v"(old), [s0] "=&v"(s0), [s1] "=&v"(s1), [s2] "=&v"(s2), [s3] "=&v"(s3)
        : [ca] "v"(cnt_a), [one] "v"(1u), [ra] "v"(rd_a)
        : "memory");
    if (__builtin_amdgcn_readfirstlane(old) == NWAVE - 1) {
      const f32x4 u = (s0 + s1) + (s2 + s3);
      PUBLISH(u[0] + u[2], u[1] + u[3], o_so + mem * 1024 + lane * 16);
    }
  }
  {
    const int o_vo = ((lane >> 3) & 3) * 1024 + (8 * w + (lane & 7)) * 16;
    u32x4 og;
#pragma unroll 1
    for (unsigned spins = 0; spins < SPIN_CAP; ++spins) {
      asm volatile("" ::: "memory");
      og = BLD(wsr, o_vo, o_so, AUX_SC1);
      if (__all(TAG_OK(og))) break;
    }
    float o0 = __uint_as_float(og[0]), o1 = __uint_as_float(og[2]);
    o0 += dpp<ROR8>(o0);
    o1 += dpp<ROR8>(o1);
    reinterpret_cast<unsigned*>(lds + L_OB)[w * 16 + (g & 1) * 8 + (lane & 7)] = cvtpk(o0, o1);
  }
  WAVE_FENCE();
  STAMP(5);
  {
    const u32x4 oa = *reinterpret_cast<const u32x4*>(lds + L_OB + w * 16 + ((lane >> 2) & 1) * 8 + 4 * (lane >> 5));
    float y[8];
    const f32x16 z = {0.f, 0.f, 0.f, 0.f, 0.f, 0.f, 0.f, 0.f, 0.f, 0.f, 0.f, 0.f, 0.f, 0.f, 0.f, 0.f};
#pragma unroll
    for (int i = 0; i < 8; ++i)
      y[i] = __builtin_amdgcn_mfma_f32_32x32x16_bf16(__builtin_bit_cast(bf16x8, oa), __builtin_bit_cast(bf16x8, wb[i]), z, 0, 0, 0)[0];
    if ((lane & 31) < 20) {
      float* yp = &lds[L_Y + w * 320 + (lane >> 5) * 160 + 8 * (lane & 31)];
      *reinterpret_cast<f32x4*>(yp) = f32x4{y[0], y[1], y[2], y[3]};
      *reinterpret_cast<f32x4*>(yp + 4) = f32x4{y[4], y[5], y[6], y[7]};
    }
  }
  __syncthreads();
  if (tid < 160) {
    f32x2 s[8];
#pragma unroll
    for (int ww = 0; ww < 8; ++ww) s[ww] = *reinterpret_cast<const f32x2*>(&lds[L_Y + ww * 320 + 2 * tid]);
    const f32x2 t = ((s[0] + s[1]) + (s[2] + s[3])) + ((s[4] + s[5]) + (s[6] + s[7]));
    PUBLISH(t[0], t[1], y_so + (h * 4 + mem) * 2560 + tid * 16);
  }
  STAMP(6);
  const int unit = wgi & 127;
  if (w == 3 && unit < 80) {
    const int y_vo = (lane >> 3) * (4 * 2560) + unit * 128 + (lane & 7) * 16;
    __builtin_amdgcn_s_sleep(Y_DELAY);
    u32x4 yg[4];
#pragma unroll 1
    for (unsigned spins = 0; spins < SPIN_CAP; ++spins) {
      asm volatile("" ::: "memory");
#pragma unroll
      for (int i = 0; i < 4; ++i) yg[i] = BLD(wsr, y_vo + i * (8 * 4 * 2560), y_so, AUX_SC1);
      unsigned bad = 0;
#pragma unroll
      for (int i = 0; i < 4; ++i) bad |= (yg[i][1] ^ tagA) | (yg[i][3] ^ tagB);
      if (__all(bad == 0)) break;
    }
    const float s0 = (__uint_as_float(yg[0][0]) + __uint_as_float(yg[1][0])) + (__uint_as_float(yg[2][0]) + __uint_as_float(yg[3][0]));
    const float s1 = (__uint_as_float(yg[0][2]) + __uint_as_float(yg[1][2])) + (__uint_as_float(yg[2][2]) + __uint_as_float(yg[3][2]));
    float s = fold32(s0, s1);
    s = fold16(s, s);
    s += dpp<ROR8>(s);
    auto t = __builtin_amdgcn_permlane32_swap(__float_as_uint(s), __float_as_uint(s), false, false);
    const int col = (unit / 20) * 160 + (unit % 10) * 16 + 2 * lane;
    if (lane < 8) *reinterpret_cast<unsigned*>(pout + ((unit / 10) & 1) * (2 * DM) + br * DM + col) = cvtpk(__uint_as_float(t[0]), __uint_as_float(t[1]));
    if (unit == 0 && lane == 0) __builtin_amdgcn_raw_buffer_store_b128(u32x4{e0 + 1u, e1 + 1u, e2 + 1u, e3 + 1u}, wsr, WS_EPOCH + br * 256, 0, AUX_SC1);
  }
}

}

extern "C" void kernel_launch(void* const* d_in, const int* in_sizes, int n_in, void* d_out, int out_size, void* d_ws,
                              size_t ws_size, hipStream_t stream) {
  Args a;
  for (int br = 0; br < 2; ++br) {
    a.x[br] = (const char*)d_in[5 * br + 0];
    a.k[br] = (const char*)d_in[5 * br + 1];
    a.v[br] = (const char*)d_in[5 * br + 2];
    a.wq[br] = (const char*)d_in[5 * br + 3];
    a.wo[br] = (const char*)d_in[5 * br + 4];
  }
  a.ws = (char*)d_ws;
  a.out = (unsigned short*)d_out;
  hipLaunchKernelGGL(attn_fused, dim3(NWG), dim3(NTHR), 0, stream, a);
}
```

```cpp
#include <hip/hip_runtime.h>
#include <hip/hip_bf16.h>
#include <stdint.h>

namespace {

constexpr int DM = 640, NH = 32, SK = 4096, DH = 128, NB = 2;
constexpr int NSPLIT = 2;
constexpr int NWAVE = 8, NTHR = NWAVE * 64;
constexpr int KEYS_WG = SK / NSPLIT;
constexpr int NSTEP = KEYS_WG / (4 * NWAVE);
constexpr int STEP_B = NWAVE * 1024;
constexpr int RING = 16;
constexpr int NBODY = NSTEP / RING;
constexpr int FLIGHT = 4;
constexpr int NWG = 2 * NB * NH * NSPLIT;
constexpr int AUX_NT = 2, AUX_SC1 = 16;
constexpr unsigned SPIN_CAP = 1u << 16;
constexpr int OOB = 0x40000000;
constexpr int TAU = 6950;
constexpr int T_OFF = 140;
constexpr int DSHIFT = 6;
constexpr int Y_DELAY = 25;

constexpr int WS_EPOCH = 0;
constexpr int WS_Q = 4096;
constexpr int WS_ML = WS_Q + 64 * 4 * 64 * 16;
constexpr int WS_O = WS_ML + 256 * 128;
constexpr int WS_Y = WS_O + 64 * 4 * 64 * 16;
constexpr int WS_END = WS_Y + 2 * 32 * 4 * 2 * 80 * 16;

typedef unsigned u32x4 __attribute__((ext_vector_type(4)));
typedef unsigned u32x3 __attribute__((ext_vector_type(3)));
typedef float f32x4 __attribute__((ext_vector_type(4)));
typedef float f32x2 __attribute__((ext_vector_type(2)));
typedef __bf16 bf16x2 __attribute__((ext_vector_type(2)));
typedef __bf16 bf16x8 __attribute__((ext_vector_type(8)));

__device__ __forceinline__ float dot2(unsigned a, unsigned b, float acc) {
  return __builtin_amdgcn_fdot2_f32_bf16(__builtin_bit_cast(bf16x2, a), __builtin_bit_cast(bf16x2, b), acc, false);
}
__device__ __forceinline__ unsigned cvtpk(float lo, float hi) {
  bf16x2 r;
  r[0] = (__bf16)lo;
  r[1] = (__bf16)hi;
  return __builtin_bit_cast(unsigned, r);
}
template <int CTRL> __device__ __forceinline__ float dpp(float x) {
  return __builtin_bit_cast(float, __builtin_amdgcn_mov_dpp(__builtin_bit_cast(int, x), CTRL, 0xf, 0xf, true));
}
constexpr int XOR1 = 0xB1, XOR2 = 0x4E, XOR7 = 0x141, ROR8 = 0x128;
__device__ __forceinline__ unsigned perm_lo(unsigned a, unsigned b) { return __builtin_amdgcn_perm(b, a, 0x05040100u); }
__device__ __forceinline__ unsigned perm_hi(unsigned a, unsigned b) { return __builtin_amdgcn_perm(b, a, 0x07060302u); }
__device__ __forceinline__ float bf_lo(unsigned u) { return __uint_as_float(u << 16); }
__device__ __forceinline__ float bf_hi(unsigned u) { return __uint_as_float(u & 0xffff0000u); }

__device__ __forceinline__ float xrow_max(float x) {
  auto s = __builtin_amdgcn_permlane16_swap(__float_as_uint(x), __float_as_uint(x), false, false);
  x = fmaxf(__uint_as_float(s[0]), __uint_as_float(s[1]));
  auto t = __builtin_amdgcn_permlane32_swap(__float_as_uint(x), __float_as_uint(x), false, false);
  return fmaxf(__uint_as_float(t[0]), __uint_as_float(t[1]));
}
__device__ __forceinline__ float fold32(float a, float b) {
  auto t = __builtin_amdgcn_permlane32_swap(__float_as_uint(a), __float_as_uint(b), false, false);
  return __uint_as_float(t[0]) + __uint_as_float(t[1]);
}
__device__ __forceinline__ float fold16(float a, float b) {
  auto t = __builtin_amdgcn_permlane16_swap(__float_as_uint(a), __float_as_uint(b), false, false);
  return __uint_as_float(t[0]) + __uint_as_float(t[1]);
}
__device__ __forceinline__ float wave_sum(float x) {
  x += dpp<XOR1>(x); x += dpp<XOR2>(x); x += dpp<XOR7>(x); x += dpp<ROR8>(x);
  x = fold16(x, x);
  return fold32(x, x);
}
template <int N> __device__ __forceinline__ void wait_vm() {
  static_assert(N >= 0 && N < 64, "");
  __builtin_amdgcn_s_waitcnt((N & 15) | (7 << 4) | (15 << 8) | ((N >> 4) << 14));
}
__device__ __forceinline__ void wait_lvl(int lvl) {
  asm volatile(
      "s_waitcnt vmcnt(5)\n\t"
      "s_cmp_gt_i32 %0, 2\n\ts_cbranch_scc1 .Lw%=\n\ts_waitcnt vmcnt(4)\n\t"
      "s_cmp_gt_i32 %0, 1\n\ts_cbranch_scc1 .Lw%=\n\ts_waitcnt vmcnt(3)\n\t"
      "s_cmp_gt_i32 %0, 0\n\ts_cbranch_scc1 .Lw%=\n\ts_waitcnt vmcnt(2)\n"
      ".Lw%=:"
      :: "s"(lvl) : "scc", "memory");
}
__device__ __forceinline__ unsigned now_ticks() { return (unsigned)__builtin_amdgcn_s_memrealtime(); }

struct Args {
  const char* x[2];
  const char* k[2];
  const char* v[2];
  const char* wq[2];
  const char* wo[2];
  char* ws;
  unsigned short* out;
};

#define RSRC(p, n) __builtin_amdgcn_make_buffer_rsrc((void*)(p), 0, (n), 0x00020000)
#define BLD(r, vo, so, aux) __builtin_amdgcn_raw_buffer_load_b128((r), (vo), (so), (aux))
#define PUBLISH(v0, v1, off) __builtin_amdgcn_raw_buffer_store_b128(u32x4{__float_as_uint(v0), tagA, __float_as_uint(v1), tagB}, wsr, (off), 0, AUX_SC1)
#define TAG_OK(v) ((v)[1] == tagA && (v)[3] == tagB)
#define SB() __builtin_amdgcn_sched_barrier(0)
#define WAVE_FENCE() __builtin_amdgcn_fence(__ATOMIC_ACQ_REL, "wavefront")

constexpr int L_SC = 0;
constexpr int L_Y = 2048;
constexpr int L_ML = L_Y + 8 * 320;
constexpr int L_OB = L_ML + 16;
constexpr int L_CNT = L_OB + 8 * 16;
constexpr int L_ZERO = L_CNT + 4;
constexpr int L_TOT = 21 * 1024;
static_assert(L_ZERO + 4 <= L_TOT && L_ZERO % 4 == 0, "");

__device__ __forceinline__ bool last_arriver(float* lds, int i) {
  unsigned old = 0;
  if ((threadIdx.x & 63) == 0) old = __hip_atomic_fetch_add(reinterpret_cast<unsigned*>(lds + L_CNT) + i, 1u, __ATOMIC_ACQ_REL, __HIP_MEMORY_SCOPE_WORKGROUP);
  return __builtin_amdgcn_readfirstlane(old) == NWAVE - 1;
}

__global__ void __launch_bounds__(NTHR, 2) attn_fused(Args a) {
  __shared__ __attribute__((aligned(16))) float lds[L_TOT];
  const int tid = threadIdx.x, lane = tid & 63, w = __builtin_amdgcn_readfirstlane(tid >> 6);
  const int g = lane >> 4, c = lane & 15;
  const unsigned t_start = now_ticks();
  if (tid < 8) reinterpret_cast<unsigned*>(lds + L_CNT)[tid] = 0u;
  const char *px0 = a.x[0], *px1 = a.x[1], *pk0 = a.k[0], *pk1 = a.k[1], *pv0 = a.v[0], *pv1 = a.v[1], *pq0 = a.wq[0], *pq1 = a.wq[1], *po0 = a.wo[0], *po1 = a.wo[1];
  char* pws = a.ws;
  unsigned short* pout = a.out;
  asm volatile("" : "+s"(px0), "+s"(px1), "+s"(pk0), "+s"(pk1), "+s"(pv0), "+s"(pv1), "+s"(pq0), "+s"(pq1), "+s"(po0), "+s"(po1), "+s"(pws), "+s"(pout));
  const int bid = blockIdx.x, xcd = bid & 7, idx = bid >> 3;
  const int grp = xcd * 8 + (idx >> 2), mem = idx & 3;
  const int br = grp >> 5, h = grp & 31, b = mem >> 1, sp = mem & 1;
  const int wgi = grp * 4 + mem;
  const int rot = wgi & (NSTEP - 1);
  const auto wsr = RSRC(pws, WS_END);
  int q_so = WS_Q + grp * 4096, ml_so = WS_ML + (wgi & ~1) * 128, o_so = WS_O + grp * 4096, y_so = WS_Y + br * (32 * 4 * 2 * 80 * 16);
  asm volatile("" : "+s"(q_so), "+s"(ml_so), "+s"(o_so), "+s"(y_so));

  const u32x4 epoch_v = BLD(wsr, 0, WS_EPOCH + br * 256, AUX_SC1 | AUX_NT);
  const int row0 = 320 * b + 40 * w + 5 * (lane >> 3);
  u32x3 xr[2];
  {
    const auto xrs = RSRC(br ? px1 : px0, NB * DM * 2);
#pragma unroll
    for (int bb = 0; bb < 2; ++bb) xr[bb] = __builtin_amdgcn_raw_buffer_load_b96(xrs, (row0 * 2) & ~3, bb * DM * 2, AUX_NT);
  }
  const auto wqr = RSRC(br ? pq1 : pq0, DM * NH * DH * 2);
  u32x4 wq[5];
#pragma unroll
  for (int j = 0; j < 5; ++j) wq[j] = BLD(wqr, row0 * (NH * DH * 2) + 16 * (lane & 7), (h * DH + sp * 64) * 2 + j * (NH * DH * 2), AUX_NT);
  SB();
  __syncthreads();
  SB();
  const size_t kvoff = ((size_t)(b * NH + h) * SK + sp * KEYS_WG) * (DH * 2);
  const auto kr = RSRC((br ? pk1 : pk0) + kvoff, KEYS_WG * DH * 2);
  const auto vr = RSRC((br ? pv1 : pv0) + kvoff, KEYS_WG * DH * 2);
  const int kv_vo = w * 1024 + lane * 16;
#define SOFF(s) ((((s) + rot) & (NSTEP - 1)) * STEP_B)
  u32x4 ring[RING];
#pragma unroll
  for (int t = 0; t < FLIGHT; ++t) ring[t] = BLD(kr, kv_vo, SOFF(t), AUX_NT);
  SB();
  const unsigned e0 = __builtin_amdgcn_readfirstlane(epoch_v[0]), e1 = __builtin_amdgcn_readfirstlane(epoch_v[1]),
                 e2 = __builtin_amdgcn_readfirstlane(epoch_v[2]), e3 = __builtin_amdgcn_readfirstlane(epoch_v[3]);
  const unsigned epoch = e0 + (e1 - e0) * 0x10001u + (e2 - e0) * 0x101u + (e3 - e0) * 0x1001u;
  const unsigned tagA = epoch + 1u, tagB = epoch ^ 0x9E3779B9u;

  {
    f32x2 qa[2][4];
#pragma unroll
    for (int bb = 0; bb < 2; ++bb) {
      const unsigned sh = (row0 & 1) * 16;
      const unsigned x01 = __builtin_amdgcn_alignbit(xr[bb][1], xr[bb][0], sh), x23 = __builtin_amdgcn_alignbit(xr[bb][2], xr[bb][1], sh), x4 = xr[bb][2] >> sh;
      const float xv[5] = {bf_lo(x01), bf_hi(x01), bf_lo(x23), bf_hi(x23), bf_lo(x4)};
#pragma unroll
      for (int d = 0; d < 4; ++d) {
        qa[bb][d] = f32x2{0.f, 0.f};
#pragma unroll
        for (int j = 0; j < 5; ++j) qa[bb][d] = __builtin_elementwise_fma(f32x2{bf_lo(wq[j][d]), bf_hi(wq[j][d])}, f32x2{xv[j], xv[j]}, qa[bb][d]);
      }
    }
    float f[8], e[4];
#pragma unroll
    for (int i = 0; i < 8; ++i) f[i] = fold32(qa[0][i >> 1][i & 1], qa[1][i >> 1][i & 1]);
#pragma unroll
    for (int i = 0; i < 4; ++i) { e[i] = fold16(f[i], f[i + 4]); e[i] += dpp<ROR8>(e[i]); }
    if (c < 8) *reinterpret_cast<f32x4*>(&lds[L_Y + w * 128 + (g >> 1) * 64 + 8 * c + 4 * (g & 1)]) = f32x4{e[0], e[1], e[2], e[3]};
  }
  if (last_arriver(lds, 0)) {
    f32x2 s = {0.f, 0.f};
#pragma unroll
    for (int ww = 0; ww < NWAVE; ++ww) s += *reinterpret_cast<const f32x2*>(&lds[L_Y + ww * 128 + 2 * lane]);
    PUBLISH(s[0], s[1], q_so + (((lane >> 5) * 2 + b) * 64 + sp * 32 + (lane & 31)) * 16);
  }
  const auto wor = RSRC((br ? po1 : po0) + (size_t)h * DH * DM * 2, DH * DM * 2);
  const int wo_vo = (lane & 31) < 20 ? (16 * w + 8 * (lane >> 5)) * (DM * 2) + mem * 320 + 16 * (lane & 31) : OOB;
  u32x4 wo[8];
  u32x4 qg0, qg1;
  const int q_vo = (b * 128 + lane) * 16;
#pragma unroll
  for (int t = FLIGHT; t < RING; ++t) {
    wait_vm<FLIGHT - 1>();
    ring[t] = BLD(kr, kv_vo, SOFF(t), AUX_NT);
    SB();
  }
#pragma unroll
  for (int j = 0; j < 8; ++j) {
    if (j == 8 - FLIGHT) { qg0 = BLD(wsr, q_vo, q_so, AUX_SC1); qg1 = BLD(wsr, q_vo + 1024, q_so, AUX_SC1); }
    if (j >= 8 - FLIGHT) wait_vm<FLIGHT + 1>(); else wait_vm<FLIGHT - 1>();
    wo[j] = BLD(wor, wo_vo, j * (DM * 2), AUX_NT);
    SB();
  }
  if (!__all(TAG_OK(qg0) && TAG_OK(qg1))) {
#pragma unroll 1
    for (unsigned spins = 0; spins < SPIN_CAP; ++spins) {
      asm volatile("" ::: "memory");
      qg0 = BLD(wsr, q_vo, q_so, AUX_SC1); qg1 = BLD(wsr, q_vo + 1024, q_so, AUX_SC1);
      if (__all(TAG_OK(qg0) && TAG_OK(qg1))) break;
      __builtin_amdgcn_s_sleep(1);
    }
  }
  reinterpret_cast<unsigned*>(lds + L_SC)[w * 256 + lane] = cvtpk(__uint_as_float(qg0[0]) + __uint_as_float(qg1[0]), __uint_as_float(qg0[2]) + __uint_as_float(qg1[2]));
  WAVE_FENCE();
  const u32x4 qv = *reinterpret_cast<const u32x4*>(lds + L_SC + w * 256 + 4 * c);
  WAVE_FENCE();
  __builtin_amdgcn_s_barrier();

  unsigned t_ref = t_start + T_OFF + (((5 + 5 + RING) * TAU) >> 8);
  int lvl;
#define RELAG(T) { lvl = (int)(now_ticks() - t_ref - (unsigned)(((T) * TAU) >> 8)) >> DSHIFT; asm volatile("" : "+s"(lvl)); SB(); }

  float* scw = &lds[L_SC + (w * 4 + g) * NSTEP];
  float mx = -INFINITY;
#define KSTEP(T)                                                                        \
  {                                                                                     \
    float d = dot2(ring[T][0], qv[0], 0.f);                                             \
    d = dot2(ring[T][1], qv[1], d);                                                     \
    d = dot2(ring[T][2], qv[2], d);                                                     \
    d = dot2(ring[T][3], qv[3], d);                                                     \
    x[(T) & 3] = d;                                                                     \
    asm volatile("" : "+v"(x[(T) & 3]));                                                \
    SB();                                                                               \
    wait_lvl(lvl);                                                                      \
    ring[T] = BLD(nr, kv_vo, SOFF(RING * it + RING + (T)), AUX_NT);                     \
    SB();                                                                               \
    if (((T) & 3) == 3) {     \
      const float a0 = x[0] + dpp<ROR8>(x[0]), a1 = x[1] + dpp<ROR8>(x[1]), a2 = x[2] + dpp<ROR8>(x[2]), a3 = x[3] + dpp<ROR8>(x[3]); \
      const float b0 = (c & 8) ? a2 : a0, b1 = (c & 8) ? a3 : a1;                       \
      const float c0 = b0 + dpp<XOR7>(b0), c1 = b1 + dpp<XOR7>(b1);                     \
      float s = (c & 4) ? c1 : c0;                                                      \
      s += dpp<XOR2>(s);                                                                \
      s += dpp<XOR1>(s);                                                                \
      mx = fmaxf(mx, s);                                                                \
      scw[RING * it + (T) - 3 + (c >> 2)] = s;                                          \
      SB();                                                                             \
      if ((T) != 15) RELAG((T) + 1)                                                     \
    }                                                                                   \
  }
#pragma unroll 1
  for (int it = 0; it < NBODY; ++it) {
    const auto nr = (it == NBODY - 1) ? vr : kr;
    float x[4];
    RELAG(0)
    KSTEP(0) KSTEP(1) KSTEP(2) KSTEP(3) KSTEP(4) KSTEP(5) KSTEP(6) KSTEP(7)
    KSTEP(8) KSTEP(9) KSTEP(10) KSTEP(11) KSTEP(12) KSTEP(13) KSTEP(14) KSTEP(15)
    t_ref += (RING * TAU) >> 8;
  }
#undef KSTEP
  constexpr float C2 = 0.088388347648318440f * 1.4426950408889634f;
  mx = fmaxf(mx, dpp<ROR8>(mx));
  mx = fmaxf(mx, dpp<XOR7>(mx));
  mx = xrow_max(mx) * C2;
  WAVE_FENCE();
  {
    f32x4 s4 = *reinterpret_cast<const f32x4*>(scw + 4 * c);
#pragma unroll
    for (int t = 0; t < 4; ++t) s4[t] = __builtin_amdgcn_exp2f(fmaf(s4[t], C2, -mx));
    *reinterpret_cast<f32x4*>(scw + 4 * c) = s4;
    const float ls = wave_sum((s4[0] + s4[1]) + (s4[2] + s4[3]));
    if (lane == 0) *reinterpret_cast<f32x2*>(&lds[L_ML + 2 * w]) = f32x2{mx, ls};
  }
  if (last_arriver(lds, 1)) {
    f32x4 ml[4];
#pragma unroll
    for (int i = 0; i < 4; ++i) ml[i] = *reinterpret_cast<const f32x4*>(&lds[L_ML + 4 * i]);
    const float mwg = fmaxf(fmaxf(fmaxf(ml[0][0], ml[0][2]), fmaxf(ml[1][0], ml[1][2])), fmaxf(fmaxf(ml[2][0], ml[2][2]), fmaxf(ml[3][0], ml[3][2])));
    float lwg = 0.f;
#pragma unroll
    for (int i = 0; i < 4; ++i) lwg += ml[i][1] * __builtin_amdgcn_exp2f(ml[i][0] - mwg) + ml[i][3] * __builtin_amdgcn_exp2f(ml[i][2] - mwg);
    if (lane == 0) PUBLISH(mwg, lwg, ml_so + sp * 128);
  }
  u32x4 wb[8];
#pragma unroll
  for (int i = 0; i < 8; ++i)
#pragma unroll
    for (int jj = 0; jj < 4; ++jj) wb[i][jj] = (i & 1) ? perm_hi(wo[2 * jj][i >> 1], wo[2 * jj + 1][i >> 1]) : perm_lo(wo[2 * jj][i >> 1], wo[2 * jj + 1][i >> 1]);
#pragma unroll
  for (int i = 0; i < 8; ++i) asm volatile("" : "+v"(wb[i]));
  SB();

  f32x2 acc2[4];
#pragma unroll
  for (int i = 0; i < 4; ++i) acc2[i] = f32x2{0.f, 0.f};
  u32x4 mlp;
  const int ml_vo = (lane & 1) * 128;
#define VSTEP(T, LOAD)                                                                  \
  {                                                                                     \
    if (((T) & 3) == 0) p4 = *reinterpret_cast<const f32x4*>(scw + RING * it + (T));    \
    _Pragma("unroll") for (int d = 0; d < 4; ++d)                                       \
      acc2[d] = __builtin_elementwise_fma(f32x2{bf_lo(ring[T][d]), bf_hi(ring[T][d])}, f32x2{p4[(T) & 3], p4[(T) & 3]}, acc2[d]); \
    asm volatile("" : "+v"(acc2[0]), "+v"(acc2[1]), "+v"(acc2[2]), "+v"(acc2[3]));     \
    SB();                                                                               \
    if (LOAD) { wait_lvl(lvl); ring[T] = BLD(vr, kv_vo, SOFF(RING * it + RING + (T)), AUX_NT); } \
    SB();                                                                               \
    if (LOAD && ((T) & 3) == 3 && (T) != 15) RELAG((T) + 1)                             \
  }
#define VBODY(LOAD) { f32x4 p4;                                                         \
    VSTEP(0, LOAD) VSTEP(1, LOAD) VSTEP(2, LOAD) VSTEP(3, LOAD) VSTEP(4, LOAD) VSTEP(5, LOAD) VSTEP(6, LOAD) VSTEP(7, LOAD) \
    VSTEP(8, LOAD) VSTEP(9, LOAD) VSTEP(10, LOAD) VSTEP(11, LOAD) VSTEP(12, LOAD) VSTEP(13, LOAD) VSTEP(14, LOAD) VSTEP(15, LOAD) }
#pragma unroll 1
  for (int it = 0; it < NBODY - 1; ++it) {
    mlp = BLD(wsr, it == NBODY - 2 ? ml_vo : OOB, ml_so, AUX_SC1);
    SB();
    RELAG(0)
    VBODY(true)
    t_ref += (RING * TAU) >> 8;
  }
  if (!__all(TAG_OK(mlp))) {
#pragma unroll 1
    for (unsigned spins = 0; spins < SPIN_CAP; ++spins) {
      asm volatile("" ::: "memory");
      mlp = BLD(wsr, ml_vo, ml_so, AUX_SC1);
      if (__all(TAG_OK(mlp))) break;
      __builtin_amdgcn_s_sleep(1);
    }
  }
  float scale;
  {
    const float m1 = __uint_as_float(mlp[0]), l1 = __uint_as_float(mlp[2]);
    const float M = fmaxf(m1, dpp<XOR1>(m1));
    const float t = l1 * __builtin_amdgcn_exp2f(m1 - M);
    scale = __builtin_amdgcn_exp2f(mx - M) / (t + dpp<XOR1>(t));
  }
  int oa_off = ((g & 1) == ((c >> 2) & 1)) ? L_OB + w * 16 + (c >> 3) * 8 + 4 * (g >> 1) : L_ZERO;
  asm volatile("" : "+v"(scale), "+v"(oa_off));
  SB();
  { const int it = NBODY - 1; VBODY(false) }
#undef VBODY
#undef VSTEP
  WAVE_FENCE();
  {
    const float f0 = fold32(acc2[0][0], acc2[2][0]), f1 = fold32(acc2[0][1], acc2[2][1]), f2 = fold32(acc2[1][0], acc2[3][0]), f3 = fold32(acc2[1][1], acc2[3][1]);
    const float r0 = fold16(f0, f2), r1 = fold16(f1, f3);
    *reinterpret_cast<f32x2*>(&lds[L_SC + w * 256 + 8 * c + 2 * (g & 1) + 4 * (g >> 1)]) = f32x2{r0 * scale, r1 * scale};
    typedef __attribute__((address_space(3))) float lds_f;
    const unsigned cnt_a = (unsigned)(size_t)(lds_f*)&lds[L_CNT + 2], rd_a = (unsigned)(size_t)(lds_f*)&lds[L_SC + 2 * lane];
    unsigned old;
    unsigned long long sv;
    f32x4 s0, s1, s2, s3;
    asm volatile(
        "s_mov_b64 %[sv], exec\n\ts_mov_b64 exec, 1\n\t"
        "ds_add_rtn_u32 %[old], %[ca], %[one]\n\t"
        "s_mov_b64 exec, %[sv]\n\t"
        "ds_read2st64_b64 %[s0], %[ra] offset1:2\n\t"
        "ds_read2st64_b64 %[s1], %[ra] offset0:4 offset1:6\n\t"
        "ds_read2st64_b64 %[s2], %[ra] offset0:8 offset1:10\n\t"
        "ds_read2st64_b64 %[s3], %[ra] offset0:12 offset1:14\n\t"
        "s_waitcnt lgkmcnt(0)"
        : [sv] "=&s"(sv), [old] "=&v"(old), [s0] "=&v"(s0), [s1] "=&v"(s1), [s2] "=&v"(s2), [s3] "=&v"(s3)
        : [ca] "v"(cnt_a), [one] "v"(1u), [ra] "v"(rd_a)
        : "memory");
    if (__builtin_amdgcn_readfirstlane(old) == NWAVE - 1) {
      const f32x4 u = (s0 + s1) + (s2 + s3);
      PUBLISH(u[0] + u[2], u[1] + u[3], o_so + mem * 1024 + lane * 16);
    }
  }
  {
    const int o_vo = ((lane >> 3) & 3) * 1024 + (8 * w + (lane & 7)) * 16;
    u32x4 og;
#pragma unroll 1
    for (unsigned spins = 0; spins < SPIN_CAP; ++spins) {
      asm volatile("" ::: "memory");
      og = BLD(wsr, o_vo, o_so, AUX_SC1);
      if (__all(TAG_OK(og))) break;
    }
    float o0 = __uint_as_float(og[0]), o1 = __uint_as_float(og[2]);
    o0 += dpp<ROR8>(o0);
    o1 += dpp<ROR8>(o1);
    reinterpret_cast<unsigned*>(lds + L_OB)[w * 16 + (g & 1) * 8 + (lane & 7)] = cvtpk(o0, o1);
  }
  WAVE_FENCE();
  {
    const u32x4 oa = *reinterpret_cast<const u32x4*>(lds + oa_off);
    float y[8];
#pragma unroll
    for (int i = 0; i < 8; ++i)
      y[i] = __builtin_amdgcn_mfma_f32_16x16x32_bf16(__builtin_bit_cast(bf16x8, oa), __builtin_bit_cast(bf16x8, wb[i]), f32x4{0.f, 0.f, 0.f, 0.f}, 0, 0, 0)[0];
    if ((lane & 31) < 20) {
      float* yp = &lds[L_Y + w * 320 + (lane >> 5) * 160 + 8 * (lane & 31)];
      *reinterpret_cast<f32x4*>(yp) = f32x4{y[0], y[1], y[2], y[3]};
      *reinterpret_cast<f32x4*>(yp + 4) = f32x4{y[4], y[5], y[6], y[7]};
    }
  }
  __syncthreads();
  if (tid < 160) {
    f32x2 s[8];
#pragma unroll
    for (int ww = 0; ww < 8; ++ww) s[ww] = *reinterpret_cast<const f32x2*>(&lds[L_Y + ww * 320 + 2 * tid]);
    const f32x2 t = ((s[0] + s[1]) + (s[2] + s[3])) + ((s[4] + s[5]) + (s[6] + s[7]));
    PUBLISH(t[0], t[1], y_so + (h * 4 + mem) * 2560 + tid * 16);
  }
  const int unit = wgi & 127;
  if (w == 3 && unit < 80) {
    const int y_vo = (lane >> 3) * (4 * 2560) + unit * 128 + (lane & 7) * 16;
    __builtin_amdgcn_s_sleep(Y_DELAY);
    u32x4 yg[4];
#pragma unroll 1
    for (unsigned spins = 0; spins < SPIN_CAP; ++spins) {
      asm volatile("" ::: "memory");
#pragma unroll
      for (int i = 0; i < 4; ++i) yg[i] = BLD(wsr, y_vo + i * (8 * 4 * 2560), y_so, AUX_SC1);
      unsigned bad = 0;
#pragma unroll
      for (int i = 0; i < 4; ++i) bad |= (yg[i][1] ^ tagA) | (yg[i][3] ^ tagB);
      if (__all(bad == 0)) break;
    }
    const float s0 = (__uint_as_float(yg[0][0]) + __uint_as_float(yg[1][0])) + (__uint_as_float(yg[2][0]) + __uint_as_float(yg[3][0]));
    const float s1 = (__uint_as_float(yg[0][2]) + __uint_as_float(yg[1][2])) + (__uint_as_float(yg[2][2]) + __uint_as_float(yg[3][2]));
    float s = fold32(s0, s1);
    s = fold16(s, s);
    s += dpp<ROR8>(s);
    auto t = __builtin_amdgcn_permlane32_swap(__float_as_uint(s), __float_as_uint(s), false, false);
    const int col = (unit / 20) * 160 + (unit % 10) * 16 + 2 * lane;
    if (lane < 8) *reinterpret_cast<unsigned*>(pout + ((unit / 10) & 1) * (2 * DM) + br * DM + col) = cvtpk(__uint_as_float(t[0]), __uint_as_float(t[1]));
    if (unit == 0 && lane == 0) __builtin_amdgcn_raw_buffer_store_b128(u32x4{e0 + 1u, e1 + 1u, e2 + 1u, e3 + 1u}, wsr, WS_EPOCH + br * 256, 0, AUX_SC1);
  }
}

}

extern "C" void kernel_launch(void* const* d_in, const int* in_sizes, int n_in, void* d_out, int out_size, void* d_ws,
                              size_t ws_size, hipStream_t stream) {
  Args a;
  for (int br = 0; br < 2; ++br) {
    a.x[br] = (const char*)d_in[5 * br + 0];
    a.k[br] = (const char*)d_in[5 * br + 1];
    a.v[br] = (const char*)d_in[5 * br + 2];
    a.wq[br] = (const char*)d_in[5 * br + 3];
    a.wo[br] = (const char*)d_in[5 * br + 4];
  }
  a.ws = (char*)d_ws;
  a.out = (unsigned short*)d_out;
  hipLaunchKernelGGL(attn_fused, dim3(NWG), dim3(NTHR), 0, stream, a);
}
```

```cpp
#include <hip/hip_runtime.h>
#include <hip/hip_bf16.h>
#include <stdint.h>

namespace {

constexpr int DM = 640, NH = 32, SK = 4096, DH = 128, NB = 2;
constexpr int NSPLIT = 2;
constexpr int NWAVE = 8, NTHR = NWAVE * 64;
constexpr int KEYS_WG = SK / NSPLIT;
constexpr int NSTEP = KEYS_WG / (4 * NWAVE);
constexpr int STEP_B = NWAVE * 1024;
constexpr int RING = 16;
constexpr int NBODY = NSTEP / RING;
constexpr int FLIGHT = 4;
constexpr int NWG = 2 * NB * NH * NSPLIT;
constexpr int AUX_NT = 2, AUX_SC1 = 16;
constexpr unsigned SPIN_CAP = 1u << 16;
constexpr int OOB = 0x40000000;
constexpr int TAU = 6930;
constexpr int T_OFF = 140;
constexpr int DSHIFT = 6;
constexpr int LEAD = -3;
constexpr int Y_DELAY = 25;

constexpr int WS_EPOCH = 0;
constexpr int WS_Q = 4096;
constexpr int WS_ML = WS_Q + 64 * 4 * 64 * 16;
constexpr int WS_O = WS_ML + 256 * 128;
constexpr int WS_Y = WS_O + 64 * 4 * 64 * 16;
constexpr int WS_END = WS_Y + 2 * 32 * 4 * 2 * 80 * 16;

typedef unsigned u32x4 __attribute__((ext_vector_type(4)));
typedef unsigned u32x3 __attribute__((ext_vector_type(3)));
typedef float f32x4 __attribute__((ext_vector_type(4)));
typedef float f32x2 __attribute__((ext_vector_type(2)));
typedef __bf16 bf16x2 __attribute__((ext_vector_type(2)));
typedef __bf16 bf16x8 __attribute__((ext_vector_type(8)));

__device__ __forceinline__ float dot2(unsigned a, unsigned b, float acc) {
  return __builtin_amdgcn_fdot2_f32_bf16(__builtin_bit_cast(bf16x2, a), __builtin_bit_cast(bf16x2, b), acc, false);
}
__device__ __forceinline__ unsigned cvtpk(float lo, float hi) {
  bf16x2 r;
  r[0] = (__bf16)lo;
  r[1] = (__bf16)hi;
  return __builtin_bit_cast(unsigned, r);
}
template <int CTRL> __device__ __forceinline__ float dpp(float x) {
  return __builtin_bit_cast(float, __builtin_amdgcn_mov_dpp(__builtin_bit_cast(int, x), CTRL, 0xf, 0xf, true));
}
constexpr int XOR1 = 0xB1, XOR2 = 0x4E, XOR7 = 0x141, ROR8 = 0x128;
__device__ __forceinline__ unsigned perm_lo(unsigned a, unsigned b) { return __builtin_amdgcn_perm(b, a, 0x05040100u); }
__device__ __forceinline__ unsigned perm_hi(unsigned a, unsigned b) { return __builtin_amdgcn_perm(b, a, 0x07060302u); }
__device__ __forceinline__ float bf_lo(unsigned u) { return __uint_as_float(u << 16); }
__device__ __forceinline__ float bf_hi(unsigned u) { return __uint_as_float(u & 0xffff0000u); }

__device__ __forceinline__ float xrow_max(float x) {
  auto s = __builtin_amdgcn_permlane16_swap(__float_as_uint(x), __float_as_uint(x), false, false);
  x = fmaxf(__uint_as_float(s[0]), __uint_as_float(s[1]));
  auto t = __builtin_amdgcn_permlane32_swap(__float_as_uint(x), __float_as_uint(x), false, false);
  return fmaxf(__uint_as_float(t[0]), __uint_as_float(t[1]));
}
__device__ __forceinline__ float fold32(float a, float b) {
  auto t = __builtin_amdgcn_permlane32_swap(__float_as_uint(a), __float_as_uint(b), false, false);
  return __uint_as_float(t[0]) + __uint_as_float(t[1]);
}
__device__ __forceinline__ float fold16(float a, float b) {
  auto t = __builtin_amdgcn_permlane16_swap(__float_as_uint(a), __float_as_uint(b), false, false);
  return __uint_as_float(t[0]) + __uint_as_float(t[1]);
}
__device__ __forceinline__ float wave_sum(float x) {
  x += dpp<XOR1>(x); x += dpp<XOR2>(x); x += dpp<XOR7>(x); x += dpp<ROR8>(x);
  x = fold16(x, x);
  return fold32(x, x);
}
template <int N> __device__ __forceinline__ void wait_vm() {
  static_assert(N >= 0 && N < 64, "");
  __builtin_amdgcn_s_waitcnt((N & 15) | (7 << 4) | (15 << 8) | ((N >> 4) << 14));
}
__device__ __forceinline__ void wait_lvl(int lvl) {
  asm volatile(
      "s_waitcnt vmcnt(5)\n\t"
      "s_cmp_gt_i32 %0, 2\n\ts_cbranch_scc1 .Lw%=\n\ts_waitcnt vmcnt(4)\n\t"
      "s_cmp_gt_i32 %0, 1\n\ts_cbranch_scc1 .Lw%=\n\ts_waitcnt vmcnt(3)\n\t"
      "s_cmp_gt_i32 %0, 0\n\ts_cbranch_scc1 .Lw%=\n\ts_waitcnt vmcnt(2)\n\t"
      "s_cmp_gt_i32 %0, %1\n\ts_cbranch_scc1 .Lw%=\n\ts_waitcnt vmcnt(1)\n"
      ".Lw%=:"
      :: "s"(lvl), "i"(LEAD) : "scc", "memory");
}
__device__ __forceinline__ unsigned now_ticks() { return (unsigned)__builtin_amdgcn_s_memrealtime(); }

struct Args {
  const char* x[2];
  const char* k[2];
  const char* v[2];
  const char* wq[2];
  const char* wo[2];
  char* ws;
  unsigned short* out;
};

#define RSRC(p, n) __builtin_amdgcn_make_buffer_rsrc((void*)(p), 0, (n), 0x00020000)
#define BLD(r, vo, so, aux) __builtin_amdgcn_raw_buffer_load_b128((r), (vo), (so), (aux))
#define PUBLISH(v0, v1, off) __builtin_amdgcn_raw_buffer_store_b128(u32x4{__float_as_uint(v0), tagA, __float_as_uint(v1), tagB}, wsr, (off), 0, AUX_SC1)
#define TAG_OK(v) ((v)[1] == tagA && (v)[3] == tagB)
#define SB() __builtin_amdgcn_sched_barrier(0)
#define WAVE_FENCE() __builtin_amdgcn_fence(__ATOMIC_ACQ_REL, "wavefront")

constexpr int L_SC = 0;
constexpr int L_Y = 2048;
constexpr int L_ML = L_Y + 8 * 320;
constexpr int L_OB = L_ML + 16;
constexpr int L_CNT = L_OB + 8 * 16;
constexpr int L_ZERO = L_CNT + 4;
constexpr int L_TOT = 21 * 1024;
static_assert(L_ZERO + 4 <= L_TOT && L_ZERO % 4 == 0, "");

__device__ __forceinline__ bool last_arriver(float* lds, int i) {
  unsigned old = 0;
  if ((threadIdx.x & 63) == 0) old = __hip_atomic_fetch_add(reinterpret_cast<unsigned*>(lds + L_CNT) + i, 1u, __ATOMIC_ACQ_REL, __HIP_MEMORY_SCOPE_WORKGROUP);
  return __builtin_amdgcn_readfirstlane(old) == NWAVE - 1;
}

__global__ void __launch_bounds__(NTHR, 2) attn_fused(Args a) {
  __shared__ __attribute__((aligned(16))) float lds[L_TOT];
  const int tid = threadIdx.x, lane = tid & 63, w = __builtin_amdgcn_readfirstlane(tid >> 6);
  const int g = lane >> 4, c = lane & 15;
  const unsigned t_start = now_ticks();
  if (tid < 8) reinterpret_cast<unsigned*>(lds + L_CNT)[tid] = 0u;
  const char *px0 = a.x[0], *px1 = a.x[1], *pk0 = a.k[0], *pk1 = a.k[1], *pv0 = a.v[0], *pv1 = a.v[1], *pq0 = a.wq[0], *pq1 = a.wq[1], *po0 = a.wo[0], *po1 = a.wo[1];
  char* pws = a.ws;
  unsigned short* pout = a.out;
  asm volatile("" : "+s"(px0), "+s"(px1), "+s"(pk0), "+s"(pk1), "+s"(pv0), "+s"(pv1), "+s"(pq0), "+s"(pq1), "+s"(po0), "+s"(po1), "+s"(pws), "+s"(pout));
  const int bid = blockIdx.x, xcd = bid & 7, idx = bid >> 3;
  const int grp = xcd * 8 + (idx >> 2), mem = idx & 3;
  const int br = grp >> 5, h = grp & 31, b = mem >> 1, sp = mem & 1;
  const int wgi = grp * 4 + mem;
  const int rot = wgi & (NSTEP - 1);
  const auto wsr = RSRC(pws, WS_END);
  int q_so = WS_Q + grp * 4096, ml_so = WS_ML + (wgi & ~1) * 128, o_so = WS_O + grp * 4096, y_so = WS_Y + br * (32 * 4 * 2 * 80 * 16);
  asm volatile("" : "+s"(q_so), "+s"(ml_so), "+s"(o_so), "+s"(y_so));

  const u32x4 epoch_v = BLD(wsr, 0, WS_EPOCH + br * 256, AUX_SC1 | AUX_NT);
  const int row0 = 320 * b + 40 * w + 5 * (lane >> 3);
  u32x3 xr[2];
  {
    const auto xrs = RSRC(br ? px1 : px0, NB * DM * 2);
#pragma unroll
    for (int bb = 0; bb < 2; ++bb) xr[bb] = __builtin_amdgcn_raw_buffer_load_b96(xrs, (row0 * 2) & ~3, bb * DM * 2, AUX_NT);
  }
  const auto wqr = RSRC(br ? pq1 : pq0, DM * NH * DH * 2);
  u32x4 wq[5];
#pragma unroll
  for (int j = 0; j < 5; ++j) wq[j] = BLD(wqr, row0 * (NH * DH * 2) + 16 * (lane & 7), (h * DH + sp * 64) * 2 + j * (NH * DH * 2), AUX_NT);
  SB();
  __syncthreads();
  SB();
  const size_t kvoff = ((size_t)(b * NH + h) * SK + sp * KEYS_WG) * (DH * 2);
  const auto kr = RSRC((br ? pk1 : pk0) + kvoff, KEYS_WG * DH * 2);
  const auto vr = RSRC((br ? pv1 : pv0) + kvoff, KEYS_WG * DH * 2);
  const int kv_vo = w * 1024 + lane * 16;
#define SOFF(s) ((((s) + rot) & (NSTEP - 1)) * STEP_B)
  u32x4 ring[RING];
#pragma unroll
  for (int t = 0; t < FLIGHT; ++t) ring[t] = BLD(kr, kv_vo, SOFF(t), AUX_NT);
  SB();
  const unsigned e0 = __builtin_amdgcn_readfirstlane(epoch_v[0]), e1 = __builtin_amdgcn_readfirstlane(epoch_v[1]),
                 e2 = __builtin_amdgcn_readfirstlane(epoch_v[2]), e3 = __builtin_amdgcn_readfirstlane(epoch_v[3]);
  const unsigned epoch = e0 + (e1 - e0) * 0x10001u + (e2 - e0) * 0x101u + (e3 - e0) * 0x1001u;
  const unsigned tagA = epoch + 1u, tagB = epoch ^ 0x9E3779B9u;

  {
    f32x2 qa[2][4];
#pragma unroll
    for (int bb = 0; bb < 2; ++bb) {
      const unsigned sh = (row0 & 1) * 16;
      const unsigned x01 = __builtin_amdgcn_alignbit(xr[bb][1], xr[bb][0], sh), x23 = __builtin_amdgcn_alignbit(xr[bb][2], xr[bb][1], sh), x4 = xr[bb][2] >> sh;
      const float xv[5] = {bf_lo(x01), bf_hi(x01), bf_lo(x23), bf_hi(x23), bf_lo(x4)};
#pragma unroll
      for (int d = 0; d < 4; ++d) {
        qa[bb][d] = f32x2{0.f, 0.f};
#pragma unroll
        for (int j = 0; j < 5; ++j) qa[bb][d] = __builtin_elementwise_fma(f32x2{bf_lo(wq[j][d]), bf_hi(wq[j][d])}, f32x2{xv[j], xv[j]}, qa[bb][d]);
      }
    }
    float f[8], e[4];
#pragma unroll
    for (int i = 0; i < 8; ++i) f[i] = fold32(qa[0][i >> 1][i & 1], qa[1][i >> 1][i & 1]);
#pragma unroll
    for (int i = 0; i < 4; ++i) { e[i] = fold16(f[i], f[i + 4]); e[i] += dpp<ROR8>(e[i]); }
    if (c < 8) *reinterpret_cast<f32x4*>(&lds[L_Y + w * 128 + (g >> 1) * 64 + 8 * c + 4 * (g & 1)]) = f32x4{e[0], e[1], e[2], e[3]};
  }
  if (last_arriver(lds, 0)) {
    f32x2 s = {0.f, 0.f};
#pragma unroll
    for (int ww = 0; ww < NWAVE; ++ww) s += *reinterpret_cast<const f32x2*>(&lds[L_Y + ww * 128 + 2 * lane]);
    PUBLISH(s[0], s[1], q_so + (((lane >> 5) * 2 + b) * 64 + sp * 32 + (lane & 31)) * 16);
  }
  const auto wor = RSRC((br ? po1 : po0) + (size_t)h * DH * DM * 2, DH * DM * 2);
  const int wo_vo = (lane & 31) < 20 ? (16 * w + 8 * (lane >> 5)) * (DM * 2) + mem * 320 + 16 * (lane & 31) : OOB;
  u32x4 wo[8];
  u32x4 qg0, qg1;
  const int q_vo = (b * 128 + lane) * 16;
#pragma unroll
  for (int t = FLIGHT; t < RING; ++t) {
    wait_vm<FLIGHT - 1>();
    ring[t] = BLD(kr, kv_vo, SOFF(t), AUX_NT);
    SB();
  }
#pragma unroll
  for (int j = 0; j < 8; ++j) {
    if (j == 8 - FLIGHT) { qg0 = BLD(wsr, q_vo, q_so, AUX_SC1); qg1 = BLD(wsr, q_vo + 1024, q_so, AUX_SC1); }
    if (j >= 8 - FLIGHT) wait_vm<FLIGHT + 1>(); else wait_vm<FLIGHT - 1>();
    wo[j] = BLD(wor, wo_vo, j * (DM * 2), AUX_NT);
    SB();
  }
  if (!__all(TAG_OK(qg0) && TAG_OK(qg1))) {
#pragma unroll 1
    for (unsigned spins = 0; spins < SPIN_CAP; ++spins) {
      asm volatile("" ::: "memory");
      qg0 = BLD(wsr, q_vo, q_so, AUX_SC1); qg1 = BLD(wsr, q_vo + 1024, q_so, AUX_SC1);
      if (__all(TAG_OK(qg0) && TAG_OK(qg1))) break;
      __builtin_amdgcn_s_sleep(1);
    }
  }
  reinterpret_cast<unsigned*>(lds + L_SC)[w * 256 + lane] = cvtpk(__uint_as_float(qg0[0]) + __uint_as_float(qg1[0]), __uint_as_float(qg0[2]) + __uint_as_float(qg1[2]));
  WAVE_FENCE();
  const u32x4 qv = *reinterpret_cast<const u32x4*>(lds + L_SC + w * 256 + 4 * c);
  WAVE_FENCE();
  __builtin_amdgcn_s_barrier();

  unsigned t_ref = t_start + T_OFF + (((5 + 5 + RING) * TAU) >> 8);
  int lvl;
#define RELAG(T) { lvl = (int)(now_ticks() - t_ref - (unsigned)(((T) * TAU) >> 8)) >> DSHIFT; asm volatile("" : "+s"(lvl)); SB(); }

  float* scw = &lds[L_SC + (w * 4 + g) * NSTEP];
  float mx = -INFINITY;
#define KSTEP(T)                                                                        \
  {                                                                                     \
    float d = dot2(ring[T][0], qv[0], 0.f);                                             \
    d = dot2(ring[T][1], qv[1], d);                                                     \
    d = dot2(ring[T][2], qv[2], d);                                                     \
    d = dot2(ring[T][3], qv[3], d);                                                     \
    x[(T) & 3] = d;                                                                     \
    asm volatile("" : "+v"(x[(T) & 3]));                                                \
    SB();                                                                               \
    wait_lvl(lvl);                                                                      \
    ring[T] = BLD(nr, kv_vo, SOFF(RING * it + RING + (T)), AUX_NT);                     \
    SB();                                                                               \
    if (((T) & 3) == 3) {     \
      const float a0 = x[0] + dpp<ROR8>(x[0]), a1 = x[1] + dpp<ROR8>(x[1]), a2 = x[2] + dpp<ROR8>(x[2]), a3 = x[3] + dpp<ROR8>(x[3]); \
      const float b0 = (c & 8) ? a2 : a0, b1 = (c & 8) ? a3 : a1;                       \
      const float c0 = b0 + dpp<XOR7>(b0), c1 = b1 + dpp<XOR7>(b1);                     \
      float s = (c & 4) ? c1 : c0;                                                      \
      s += dpp<XOR2>(s);                                                                \
      s += dpp<XOR1>(s);                                                                \
      mx = fmaxf(mx, s);                                                                \
      scw[RING * it + (T) - 3 + (c >> 2)] = s;                                          \
      SB();                                                                             \
      if ((T) != 15) RELAG((T) + 1)                                                     \
    }                                                                                   \
  }
#pragma unroll 1
  for (int it = 0; it < NBODY; ++it) {
    const auto nr = (it == NBODY - 1) ? vr : kr;
    float x[4];
    RELAG(0)
    KSTEP(0) KSTEP(1) KSTEP(2) KSTEP(3) KSTEP(4) KSTEP(5) KSTEP(6) KSTEP(7)
    KSTEP(8) KSTEP(9) KSTEP(10) KSTEP(11) KSTEP(12) KSTEP(13) KSTEP(14) KSTEP(15)
    t_ref += (RING * TAU) >> 8;
  }
#undef KSTEP
  constexpr float C2 = 0.088388347648318440f * 1.4426950408889634f;
  mx = fmaxf(mx, dpp<ROR8>(mx));
  mx = fmaxf(mx, dpp<XOR7>(mx));
  mx = xrow_max(mx) * C2;
  WAVE_FENCE();
  {
    f32x4 s4 = *reinterpret_cast<const f32x4*>(scw + 4 * c);
#pragma unroll
    for (int t = 0; t < 4; ++t) s4[t] = __builtin_amdgcn_exp2f(fmaf(s4[t], C2, -mx));
    *reinterpret_cast<f32x4*>(scw + 4 * c) = s4;
    const float ls = wave_sum((s4[0] + s4[1]) + (s4[2] + s4[3]));
    if (lane == 0) *reinterpret_cast<f32x2*>(&lds[L_ML + 2 * w]) = f32x2{mx, ls};
  }
  if (last_arriver(lds, 1)) {
    f32x4 ml[4];
#pragma unroll
    for (int i = 0; i < 4; ++i) ml[i] = *reinterpret_cast<const f32x4*>(&lds[L_ML + 4 * i]);
    const float mwg = fmaxf(fmaxf(fmaxf(ml[0][0], ml[0][2]), fmaxf(ml[1][0], ml[1][2])), fmaxf(fmaxf(ml[2][0], ml[2][2]), fmaxf(ml[3][0], ml[3][2])));
    float lwg = 0.f;
#pragma unroll
    for (int i = 0; i < 4; ++i) lwg += ml[i][1] * __builtin_amdgcn_exp2f(ml[i][0] - mwg) + ml[i][3] * __builtin_amdgcn_exp2f(ml[i][2] - mwg);
    if (lane == 0) PUBLISH(mwg, lwg, ml_so + sp * 128);
  }
  u32x4 wb[8];
#pragma unroll
  for (int i = 0; i < 8; ++i)
#pragma unroll
    for (int jj = 0; jj < 4; ++jj) wb[i][jj] = (i & 1) ? perm_hi(wo[2 * jj][i >> 1], wo[2 * jj + 1][i >> 1]) : perm_lo(wo[2 * jj][i >> 1], wo[2 * jj + 1][i >> 1]);
#pragma unroll
  for (int i = 0; i < 8; ++i) asm volatile("" : "+v"(wb[i]));
  SB();

  f32x2 acc2[4];
#pragma unroll
  for (int i = 0; i < 4; ++i) acc2[i] = f32x2{0.f, 0.f};
  u32x4 mlp;
  const int ml_vo = (lane & 1) * 128;
#define VSTEP(T, LOAD)                                                                  \
  {                                                                                     \
    if (((T) & 3) == 0) p4 = *reinterpret_cast<const f32x4*>(scw + RING * it + (T));    \
    _Pragma("unroll") for (int d = 0; d < 4; ++d)                                       \
      acc2[d] = __builtin_elementwise_fma(f32x2{bf_lo(ring[T][d]), bf_hi(ring[T][d])}, f32x2{p4[(T) & 3], p4[(T) & 3]}, acc2[d]); \
    asm volatile("" : "+v"(acc2[0]), "+v"(acc2[1]), "+v"(acc2[2]), "+v"(acc2[3]));     \
    SB();                                                                               \
    if (LOAD) { wait_lvl(lvl); ring[T] = BLD(vr, kv_vo, SOFF(RING * it + RING + (T)), AUX_NT); } \
    SB();                                                                               \
    if (LOAD && ((T) & 3) == 3 && (T) != 15) RELAG((T) + 1)                             \
  }
#define VBODY(LOAD) { f32x4 p4;                                                         \
    VSTEP(0, LOAD) VSTEP(1, LOAD) VSTEP(2, LOAD) VSTEP(3, LOAD) VSTEP(4, LOAD) VSTEP(5, LOAD) VSTEP(6, LOAD) VSTEP(7, LOAD) \
    VSTEP(8, LOAD) VSTEP(9, LOAD) VSTEP(10, LOAD) VSTEP(11, LOAD) VSTEP(12, LOAD) VSTEP(13, LOAD) VSTEP(14, LOAD) VSTEP(15, LOAD) }
#pragma unroll 1
  for (int it = 0; it < NBODY - 1; ++it) {
    mlp = BLD(wsr, it == NBODY - 2 ? ml_vo : OOB, ml_so, AUX_SC1);
    SB();
    RELAG(0)
    VBODY(true)
    t_ref += (RING * TAU) >> 8;
  }
  if (!__all(TAG_OK(mlp))) {
#pragma unroll 1
    for (unsigned spins = 0; spins < SPIN_CAP; ++spins) {
      asm volatile("" ::: "memory");
      mlp = BLD(wsr, ml_vo, ml_so, AUX_SC1);
      if (__all(TAG_OK(mlp))) break;
      __builtin_amdgcn_s_sleep(1);
    }
  }
  float scale;
  {
    const float m1 = __uint_as_float(mlp[0]), l1 = __uint_as_float(mlp[2]);
    const float M = fmaxf(m1, dpp<XOR1>(m1));
    const float t = l1 * __builtin_amdgcn_exp2f(m1 - M);
    scale = __builtin_amdgcn_exp2f(mx - M) / (t + dpp<XOR1>(t));
  }
  int oa_off = ((g & 1) == ((c >> 2) & 1)) ? L_OB + w * 16 + (c >> 3) * 8 + 4 * (g >> 1) : L_ZERO;
  asm volatile("" : "+v"(scale), "+v"(oa_off));
  SB();
  { const int it = NBODY - 1; VBODY(false) }
#undef VBODY
#undef VSTEP
  WAVE_FENCE();
  {
    const float f0 = fold32(acc2[0][0], acc2[2][0]), f1 = fold32(acc2[0][1], acc2[2][1]), f2 = fold32(acc2[1][0], acc2[3][0]), f3 = fold32(acc2[1][1], acc2[3][1]);
    const float r0 = fold16(f0, f2), r1 = fold16(f1, f3);
    *reinterpret_cast<f32x2*>(&lds[L_SC + w * 256 + 8 * c + 2 * (g & 1) + 4 * (g >> 1)]) = f32x2{r0 * scale, r1 * scale};
    typedef __attribute__((address_space(3))) float lds_f;
    const unsigned cnt_a = (unsigned)(size_t)(lds_f*)&lds[L_CNT + 2], rd_a = (unsigned)(size_t)(lds_f*)&lds[L_SC + 2 * lane];
    unsigned old;
    unsigned long long sv;
    f32x4 s0, s1, s2, s3;
    asm volatile(
        "s_mov_b64 %[sv], exec\n\ts_mov_b64 exec, 1\n\t"
        "ds_add_rtn_u32 %[old], %[ca], %[one]\n\t"
        "s_mov_b64 exec, %[sv]\n\t"
        "ds_read2st64_b64 %[s0], %[ra] offset1:2\n\t"
        "ds_read2st64_b64 %[s1], %[ra] offset0:4 offset1:6\n\t"
        "ds_read2st64_b64 %[s2], %[ra] offset0:8 offset1:10\n\t"
        "ds_read2st64_b64 %[s3], %[ra] offset0:12 offset1:14\n\t"
        "s_waitcnt lgkmcnt(0)"
        : [sv] "=&s"(sv), [old] "=&v"(old), [s0] "=&v"(s0), [s1] "=&v"(s1), [s2] "=&v"(s2), [s3] "=&v"(s3)
        : [ca] "v"(cnt_a), [one] "v"(1u), [ra] "v"(rd_a)
        : "memory");
    if (__builtin_amdgcn_readfirstlane(old) == NWAVE - 1) {
      const f32x4 u = (s0 + s1) + (s2 + s3);
      PUBLISH(u[0] + u[2], u[1] + u[3], o_so + mem * 1024 + lane * 16);
    }
  }
  {
    const int o_vo = ((lane >> 3) & 3) * 1024 + (8 * w + (lane & 7)) * 16;
    u32x4 og;
#pragma unroll 1
    for (unsigned spins = 0; spins < SPIN_CAP; ++spins) {
      asm volatile("" ::: "memory");
      og = BLD(wsr, o_vo, o_so, AUX_SC1);
      if (__all(TAG_OK(og))) break;
    }
    float o0 = __uint_as_float(og[0]), o1 = __uint_as_float(og[2]);
    o0 += dpp<ROR8>(o0);
    o1 += dpp<ROR8>(o1);
    reinterpret_cast<unsigned*>(lds + L_OB)[w * 16 + (g & 1) * 8 + (lane & 7)] = cvtpk(o0, o1);
  }
  WAVE_FENCE();
  {
    const u32x4 oa = *reinterpret_cast<const u32x4*>(lds + oa_off);
    float y[8];
#pragma unroll
    for (int i = 0; i < 8; ++i)
      y[i] = __builtin_amdgcn_mfma_f32_16x16x32_bf16(__builtin_bit_cast(bf16x8, oa), __builtin_bit_cast(bf16x8, wb[i]), f32x4{0.f, 0.f, 0.f, 0.f}, 0, 0, 0)[0];
    if ((lane & 31) < 20) {
      float* yp = &lds[L_Y + w * 320 + (lane >> 5) * 160 + 8 * (lane & 31)];
      *reinterpret_cast<f32x4*>(yp) = f32x4{y[0], y[1], y[2], y[3]};
      *reinterpret_cast<f32x4*>(yp + 4) = f32x4{y[4], y[5], y[6], y[7]};
    }
  }
  __syncthreads();
  if (tid < 160) {
    f32x2 s[8];
#pragma unroll
    for (int ww = 0; ww < 8; ++ww) s[ww] = *reinterpret_cast<const f32x2*>(&lds[L_Y + ww * 320 + 2 * tid]);
    const f32x2 t = ((s[0] + s[1]) + (s[2] + s[3])) + ((s[4] + s[5]) + (s[6] + s[7]));
    PUBLISH(t[0], t[1], y_so + (h * 4 + mem) * 2560 + tid * 16);
  }
  const int unit = wgi & 127;
  if (w == 3 && unit < 80) {
    const int y_vo = (lane >> 3) * (4 * 2560) + unit * 128 + (lane & 7) * 16;
    int out_vo = lane < 8 ? (((unit / 10) & 1) * (2 * DM) + br * DM + (unit / 20) * 160 + (unit % 10) * 16 + 2 * lane) * 2 : OOB;
    asm volatile("" : "+v"(out_vo));
    __builtin_amdgcn_s_sleep(Y_DELAY);
    u32x4 yg[4];
#pragma unroll 1
    for (unsigned spins = 0; spins < SPIN_CAP; ++spins) {
      asm volatile("" ::: "memory");
#pragma unroll
      for (int i = 0; i < 4; ++i) yg[i] = BLD(wsr, y_vo + i * (8 * 4 * 2560), y_so, AUX_SC1);
      unsigned bad = 0;
#pragma unroll
      for (int i = 0; i < 4; ++i) bad |= (yg[i][1] ^ tagA) | (yg[i][3] ^ tagB);
      if (__all(bad == 0)) break;
    }
    const float s0 = (__uint_as_float(yg[0][0]) + __uint_as_float(yg[1][0])) + (__uint_as_float(yg[2][0]) + __uint_as_float(yg[3][0]));
    const float s1 = (__uint_as_float(yg[0][2]) + __uint_as_float(yg[1][2])) + (__uint_as_float(yg[2][2]) + __uint_as_float(yg[3][2]));
    float s = fold32(s0, s1);
    s = fold16(s, s);
    s += dpp<ROR8>(s);
    auto t = __builtin_amdgcn_permlane32_swap(__float_as_uint(s), __float_as_uint(s), false, false);
    __builtin_amdgcn_raw_buffer_store_b32(cvtpk(__uint_as_float(t[0]), __uint_as_float(t[1])), RSRC(pout, NB * 2 * DM * 2), out_vo, 0, 0);
    if (unit == 0 && lane == 0) __builtin_amdgcn_raw_buffer_store_b128(u32x4{e0 + 1u, e1 + 1u, e2 + 1u, e3 + 1u}, wsr, WS_EPOCH + br * 256, 0, AUX_SC1);
  }
}

}

extern "C" void kernel_launch(void* const* d_in, const int* in_sizes, int n_in, void* d_out, int out_size, void* d_ws,
                              size_t ws_size, hipStream_t stream) {
  Args a;
  for (int br = 0; br < 2; ++br) {
    a.x[br] = (const char*)d_in[5 * br + 0];
    a.k[br] = (const char*)d_in[5 * br + 1];
    a.v[br] = (const char*)d_in[5 * br + 2];
    a.wq[br] = (const char*)d_in[5 * br + 3];
    a.wo[br] = (const char*)d_in[5 * br + 4];
  }
  a.ws = (char*)d_ws;
  a.out = (unsigned short*)d_out;
  hipLaunchKernelGGL(attn_fused, dim3(NWG), dim3(NTHR), 0, stream, a);
}
```

```cpp
#include <hip/hip_runtime.h>
#include <hip/hip_bf16.h>
#include <stdint.h>

namespace {

constexpr int DM = 640, NH = 32, SK = 4096, DH = 128, NB = 2;
constexpr int NSPLIT = 2;
constexpr int NWAVE = 8, NTHR = NWAVE * 64;
constexpr int KEYS_WG = SK / NSPLIT;
constexpr int NSTEP = KEYS_WG / (4 * NWAVE);
constexpr int STEP_B = NWAVE * 1024;
constexpr int RING = 16;
constexpr int NBODY = NSTEP / RING;
constexpr int FLIGHT = 4;
constexpr int NWG = 2 * NB * NH * NSPLIT;
constexpr int AUX_NT = 2, AUX_SC1 = 16;
constexpr unsigned SPIN_CAP = 1u << 16;
constexpr int OOB = 0x40000000;
constexpr int TAU = 6930;
constexpr int T_OFF = 140;
constexpr int DSHIFT = 6;
constexpr int LEAD = -3;
constexpr int Y_DELAY = 25;

constexpr int WS_EPOCH = 0;
constexpr int WS_Q = 4096;
constexpr int WS_ML = WS_Q + 64 * 4 * 64 * 16;
constexpr int WS_O = WS_ML + 256 * 128;
constexpr int WS_Y = WS_O + 64 * 4 * 64 * 16;
constexpr int WS_END = WS_Y + 2 * 32 * 4 * 2 * 80 * 16;

typedef unsigned u32x4 __attribute__((ext_vector_type(4)));
typedef unsigned u32x3 __attribute__((ext_vector_type(3)));
typedef float f32x4 __attribute__((ext_vector_type(4)));
typedef float f32x2 __attribute__((ext_vector_type(2)));
typedef __bf16 bf16x2 __attribute__((ext_vector_type(2)));
typedef __bf16 bf16x8 __attribute__((ext_vector_type(8)));

__device__ __forceinline__ float dot2(unsigned a, unsigned b, float acc) {
  return __builtin_amdgcn_fdot2_f32_bf16(__builtin_bit_cast(bf16x2, a), __builtin_bit_cast(bf16x2, b), acc, false);
}
__device__ __forceinline__ unsigned cvtpk(float lo, float hi) {
  bf16x2 r;
  r[0] = (__bf16)lo;
  r[1] = (__bf16)hi;
  return __builtin_bit_cast(unsigned, r);
}
template <int CTRL> __device__ __forceinline__ float dpp(float x) {
  return __builtin_bit_cast(float, __builtin_amdgcn_mov_dpp(__builtin_bit_cast(int, x), CTRL, 0xf, 0xf, true));
}
constexpr int XOR1 = 0xB1, XOR2 = 0x4E, XOR7 = 0x141, ROR8 = 0x128;
__device__ __forceinline__ unsigned perm_lo(unsigned a, unsigned b) { return __builtin_amdgcn_perm(b, a, 0x05040100u); }
__device__ __forceinline__ unsigned perm_hi(unsigned a, unsigned b) { return __builtin_amdgcn_perm(b, a, 0x07060302u); }
__device__ __forceinline__ float bf_lo(unsigned u) { return __uint_as_float(u << 16); }
__device__ __forceinline__ float bf_hi(unsigned u) { return __uint_as_float(u & 0xffff0000u); }

__device__ __forceinline__ float xrow_max(float x) {
  auto s = __builtin_amdgcn_permlane16_swap(__float_as_uint(x), __float_as_uint(x), false, false);
  x = fmaxf(__uint_as_float(s[0]), __uint_as_float(s[1]));
  auto t = __builtin_amdgcn_permlane32_swap(__float_as_uint(x), __float_as_uint(x), false, false);
  return fmaxf(__uint_as_float(t[0]), __uint_as_float(t[1]));
}
__device__ __forceinline__ float fold32(float a, float b) {
  auto t = __builtin_amdgcn_permlane32_swap(__float_as_uint(a), __float_as_uint(b), false, false);
  return __uint_as_float(t[0]) + __uint_as_float(t[1]);
}
__device__ __forceinline__ float fold16(float a, float b) {
  auto t = __builtin_amdgcn_permlane16_swap(__float_as_uint(a), __float_as_uint(b), false, false);
  return __uint_as_float(t[0]) + __uint_as_float(t[1]);
}
__device__ __forceinline__ float wave_sum(float x) {
  x += dpp<XOR1>(x); x += dpp<XOR2>(x); x += dpp<XOR7>(x); x += dpp<ROR8>(x);
  x = fold16(x, x);
  return fold32(x, x);
}
template <int N> __device__ __forceinline__ void wait_vm() {
  static_assert(N >= 0 && N < 64, "");
  __builtin_amdgcn_s_waitcnt((N & 15) | (7 << 4) | (15 << 8) | ((N >> 4) << 14));
}
template <int X> __device__ __forceinline__ void wait_lvl(int lvl) {
  asm volatile(
      "s_waitcnt vmcnt(%2+5)\n\t"
      "s_cmp_gt_i32 %0, 2\n\ts_cbranch_scc1 .Lw%=\n\ts_waitcnt vmcnt(%2+4)\n\t"
      "s_cmp_gt_i32 %0, 1\n\ts_cbranch_scc1 .Lw%=\n\ts_waitcnt vmcnt(%2+3)\n\t"
      "s_cmp_gt_i32 %0, 0\n\ts_cbranch_scc1 .Lw%=\n\ts_waitcnt vmcnt(%2+2)\n\t"
      "s_cmp_gt_i32 %0, %1\n\ts_cbranch_scc1 .Lw%=\n\ts_waitcnt vmcnt(%2+1)\n"
      ".Lw%=:"
      :: "s"(lvl), "i"(LEAD), "i"(X) : "scc", "memory");
}
__device__ __forceinline__ unsigned now_ticks() { return (unsigned)__builtin_amdgcn_s_memrealtime(); }

struct Args {
  const char* x[2];
  const char* k[2];
  const char* v[2];
  const char* wq[2];
  const char* wo[2];
  char* ws;
  unsigned short* out;
};

#define RSRC(p, n) __builtin_amdgcn_make_buffer_rsrc((void*)(p), 0, (n), 0x00020000)
#define BLD(r, vo, so, aux) __builtin_amdgcn_raw_buffer_load_b128((r), (vo), (so), (aux))
#define PUBLISH(v0, v1, off) __builtin_amdgcn_raw_buffer_store_b128(u32x4{__float_as_uint(v0), tagA, __float_as_uint(v1), tagB}, wsr, (off), 0, AUX_SC1)
#define TAG_OK(v) ((v)[1] == tagA && (v)[3] == tagB)
#define SB() __builtin_amdgcn_sched_barrier(0)
#define WAVE_FENCE() __builtin_amdgcn_fence(__ATOMIC_ACQ_REL, "wavefront")

constexpr int L_SC = 0;
constexpr int L_Y = 2048;
constexpr int L_ML = L_Y + 8 * 320;
constexpr int L_OB = L_ML + 16;
constexpr int L_CNT = L_OB + 8 * 16;
constexpr int L_ZERO = L_CNT + 4;
constexpr int L_TOT = 21 * 1024;
static_assert(L_ZERO + 4 <= L_TOT && L_ZERO % 4 == 0, "");

__device__ __forceinline__ bool last_arriver(float* lds, int i) {
  unsigned old = 0;
  if ((threadIdx.x & 63) == 0) old = __hip_atomic_fetch_add(reinterpret_cast<unsigned*>(lds + L_CNT) + i, 1u, __ATOMIC_ACQ_REL, __HIP_MEMORY_SCOPE_WORKGROUP);
  return __builtin_amdgcn_readfirstlane(old) == NWAVE - 1;
}

__global__ void __launch_bounds__(NTHR, 2) attn_fused(Args a) {
  __shared__ __attribute__((aligned(16))) float lds[L_TOT];
  const int tid = threadIdx.x, lane = tid & 63, w = __builtin_amdgcn_readfirstlane(tid >> 6);
  const int g = lane >> 4, c = lane & 15;
  const unsigned t_start = now_ticks();
  if (tid < 8) reinterpret_cast<unsigned*>(lds + L_CNT)[tid] = 0u;
  const char *px0 = a.x[0], *px1 = a.x[1], *pk0 = a.k[0], *pk1 = a.k[1], *pv0 = a.v[0], *pv1 = a.v[1], *pq0 = a.wq[0], *pq1 = a.wq[1], *po0 = a.wo[0], *po1 = a.wo[1];
  char* pws = a.ws;
  unsigned short* pout = a.out;
  asm volatile("" : "+s"(px0), "+s"(px1), "+s"(pk0), "+s"(pk1), "+s"(pv0), "+s"(pv1), "+s"(pq0), "+s"(pq1), "+s"(po0), "+s"(po1), "+s"(pws), "+s"(pout));
  const int bid = blockIdx.x, xcd = bid & 7, idx = bid >> 3;
  const int grp = xcd * 8 + (idx >> 2), mem = idx & 3;
  const int br = grp >> 5, h = grp & 31, b = mem >> 1, sp = mem & 1;
  const int wgi = grp * 4 + mem;
  const int rot = wgi & (NSTEP - 1);
  const auto wsr = RSRC(pws, WS_END);
  int q_so = WS_Q + grp * 4096, ml_so = WS_ML + (wgi & ~1) * 128, o_so = WS_O + grp * 4096, y_so = WS_Y + br * (32 * 4 * 2 * 80 * 16);
  asm volatile("" : "+s"(q_so), "+s"(ml_so), "+s"(o_so), "+s"(y_so));

  const u32x4 epoch_v = BLD(wsr, 0, WS_EPOCH + br * 256, AUX_SC1 | AUX_NT);
  const int row0 = 320 * b + 40 * w + 5 * (lane >> 3);
  u32x3 xr[2];
  {
    const auto xrs = RSRC(br ? px1 : px0, NB * DM * 2);
#pragma unroll
    for (int bb = 0; bb < 2; ++bb) xr[bb] = __builtin_amdgcn_raw_buffer_load_b96(xrs, (row0 * 2) & ~3, bb * DM * 2, AUX_NT);
  }
  const auto wqr = RSRC(br ? pq1 : pq0, DM * NH * DH * 2);
  u32x4 wq[5];
#pragma unroll
  for (int j = 0; j < 5; ++j) wq[j] = BLD(wqr, row0 * (NH * DH * 2) + 16 * (lane & 7), (h * DH + sp * 64) * 2 + j * (NH * DH * 2), AUX_NT);
  SB();
  __syncthreads();
  SB();
  const size_t kvoff = ((size_t)(b * NH + h) * SK + sp * KEYS_WG) * (DH * 2);
  const auto kr = RSRC((br ? pk1 : pk0) + kvoff, KEYS_WG * DH * 2);
  const auto vr = RSRC((br ? pv1 : pv0) + kvoff, KEYS_WG * DH * 2);
  const int kv_vo = w * 1024 + lane * 16;
#define SOFF(s) ((((s) + rot) & (NSTEP - 1)) * STEP_B)
  u32x4 ring[RING];
#pragma unroll
  for (int t = 0; t < FLIGHT; ++t) ring[t] = BLD(kr, kv_vo, SOFF(t), AUX_NT);
  SB();
  const unsigned e0 = __builtin_amdgcn_readfirstlane(epoch_v[0]), e1 = __builtin_amdgcn_readfirstlane(epoch_v[1]),
                 e2 = __builtin_amdgcn_readfirstlane(epoch_v[2]), e3 = __builtin_amdgcn_readfirstlane(epoch_v[3]);
  const unsigned epoch = e0 + (e1 - e0) * 0x10001u + (e2 - e0) * 0x101u + (e3 - e0) * 0x1001u;
  const unsigned tagA = epoch + 1u, tagB = epoch ^ 0x9E3779B9u;

  {
    f32x2 qa[2][4];
#pragma unroll
    for (int bb = 0; bb < 2; ++bb) {
      const unsigned sh = (row0 & 1) * 16;
      const unsigned x01 = __builtin_amdgcn_alignbit(xr[bb][1], xr[bb][0], sh), x23 = __builtin_amdgcn_alignbit(xr[bb][2], xr[bb][1], sh), x4 = xr[bb][2] >> sh;
      const float xv[5] = {bf_lo(x01), bf_hi(x01), bf_lo(x23), bf_hi(x23), bf_lo(x4)};
#pragma unroll
      for (int d = 0; d < 4; ++d) {
        qa[bb][d] = f32x2{0.f, 0.f};
#pragma unroll
        for (int j = 0; j < 5; ++j) qa[bb][d] = __builtin_elementwise_fma(f32x2{bf_lo(wq[j][d]), bf_hi(wq[j][d])}, f32x2{xv[j], xv[j]}, qa[bb][d]);
      }
    }
    float f[8], e[4];
#pragma unroll
    for (int i = 0; i < 8; ++i) f[i] = fold32(qa[0][i >> 1][i & 1], qa[1][i >> 1][i & 1]);
#pragma unroll
    for (int i = 0; i < 4; ++i) { e[i] = fold16(f[i], f[i + 4]); e[i] += dpp<ROR8>(e[i]); }
    if (c < 8) *reinterpret_cast<f32x4*>(&lds[L_Y + w * 128 + (g >> 1) * 64 + 8 * c + 4 * (g & 1)]) = f32x4{e[0], e[1], e[2], e[3]};
  }
  if (last_arriver(lds, 0)) {
    f32x2 s = {0.f, 0.f};
#pragma unroll
    for (int ww = 0; ww < NWAVE; ++ww) s += *reinterpret_cast<const f32x2*>(&lds[L_Y + ww * 128 + 2 * lane]);
    PUBLISH(s[0], s[1], q_so + (((lane >> 5) * 2 + b) * 64 + sp * 32 + (lane & 31)) * 16);
  }
  const auto wor = RSRC((br ? po1 : po0) + (size_t)h * DH * DM * 2, DH * DM * 2);
  const int wo_vo = (lane & 31) < 20 ? (16 * w + 8 * (lane >> 5)) * (DM * 2) + mem * 320 + 16 * (lane & 31) : OOB;
  u32x4 wo[8];
  u32x4 qg0, qg1;
  const int q_vo = (b * 128 + lane) * 16;
#pragma unroll
  for (int t = FLIGHT; t < RING; ++t) {
    wait_vm<FLIGHT - 1>();
    ring[t] = BLD(kr, kv_vo, SOFF(t), AUX_NT);
    SB();
  }
#pragma unroll
  for (int j = 0; j < 8; ++j) {
    if (j == 8 - FLIGHT) { qg0 = BLD(wsr, q_vo, q_so, AUX_SC1); qg1 = BLD(wsr, q_vo + 1024, q_so, AUX_SC1); }
    if (j >= 8 - FLIGHT) wait_vm<FLIGHT + 1>(); else wait_vm<FLIGHT - 1>();
    wo[j] = BLD(wor, wo_vo, j * (DM * 2), AUX_NT);
    SB();
  }
  if (!__all(TAG_OK(qg0) && TAG_OK(qg1))) {
#pragma unroll 1
    for (unsigned spins = 0; spins < SPIN_CAP; ++spins) {
      asm volatile("" ::: "memory");
      qg0 = BLD(wsr, q_vo, q_so, AUX_SC1); qg1 = BLD(wsr, q_vo + 1024, q_so, AUX_SC1);
      if (__all(TAG_OK(qg0) && TAG_OK(qg1))) break;
      __builtin_amdgcn_s_sleep(1);
    }
  }
  reinterpret_cast<unsigned*>(lds + L_SC)[w * 256 + lane] = cvtpk(__uint_as_float(qg0[0]) + __uint_as_float(qg1[0]), __uint_as_float(qg0[2]) + __uint_as_float(qg1[2]));
  WAVE_FENCE();
  const u32x4 qv = *reinterpret_cast<const u32x4*>(lds + L_SC + w * 256 + 4 * c);
  WAVE_FENCE();
  __builtin_amdgcn_s_barrier();

  unsigned t_ref = t_start + T_OFF + (((5 + 5 + RING) * TAU) >> 8);
  int lvl;
#define RELAG(T) { lvl = (int)(now_ticks() - t_ref - (unsigned)(((T) * TAU) >> 8)) >> DSHIFT; asm volatile("" : "+s"(lvl)); SB(); }

  float* scw = &lds[L_SC + (w * 4 + g) * NSTEP];
  float mx = -INFINITY;
#define KSTEP(T)                                                                        \
  {                                                                                     \
    float d = dot2(ring[T][0], qv[0], 0.f);                                             \
    d = dot2(ring[T][1], qv[1], d);                                                     \
    d = dot2(ring[T][2], qv[2], d);                                                     \
    d = dot2(ring[T][3], qv[3], d);                                                     \
    x[(T) & 3] = d;                                                                     \
    asm volatile("" : "+v"(x[(T) & 3]));                                                \
    SB();                                                                               \
    wait_lvl<0>(lvl);                                                                   \
    ring[T] = BLD(nr, kv_vo, SOFF(RING * it + RING + (T)), AUX_NT);                     \
    SB();                                                                               \
    if (((T) & 3) == 3) {     \
      const float a0 = x[0] + dpp<ROR8>(x[0]), a1 = x[1] + dpp<ROR8>(x[1]), a2 = x[2] + dpp<ROR8>(x[2]), a3 = x[3] + dpp<ROR8>(x[3]); \
      const float b0 = (c & 8) ? a2 : a0, b1 = (c & 8) ? a3 : a1;                       \
      const float c0 = b0 + dpp<XOR7>(b0), c1 = b1 + dpp<XOR7>(b1);                     \
      float s = (c & 4) ? c1 : c0;                                                      \
      s += dpp<XOR2>(s);                                                                \
      s += dpp<XOR1>(s);                                                                \
      mx = fmaxf(mx, s);                                                                \
      scw[RING * it + (T) - 3 + (c >> 2)] = s;                                          \
      SB();                                                                             \
      if ((T) != 15) RELAG((T) + 1)                                                     \
    }                                                                                   \
  }
#pragma unroll 1
  for (int it = 0; it < NBODY; ++it) {
    const auto nr = (it == NBODY - 1) ? vr : kr;
    float x[4];
    RELAG(0)
    KSTEP(0) KSTEP(1) KSTEP(2) KSTEP(3) KSTEP(4) KSTEP(5) KSTEP(6) KSTEP(7)
    KSTEP(8) KSTEP(9) KSTEP(10) KSTEP(11) KSTEP(12) KSTEP(13) KSTEP(14) KSTEP(15)
    t_ref += (RING * TAU) >> 8;
  }
#undef KSTEP
  constexpr float C2 = 0.088388347648318440f * 1.4426950408889634f;
  mx = fmaxf(mx, dpp<ROR8>(mx));
  mx = fmaxf(mx, dpp<XOR7>(mx));
  mx = xrow_max(mx) * C2;
  WAVE_FENCE();
  {
    f32x4 s4 = *reinterpret_cast<const f32x4*>(scw + 4 * c);
#pragma unroll
    for (int t = 0; t < 4; ++t) s4[t] = __builtin_amdgcn_exp2f(fmaf(s4[t], C2, -mx));
    *reinterpret_cast<f32x4*>(scw + 4 * c) = s4;
    const float ls = wave_sum((s4[0] + s4[1]) + (s4[2] + s4[3]));
    if (lane == 0) *reinterpret_cast<f32x2*>(&lds[L_ML + 2 * w]) = f32x2{mx, ls};
  }
  if (last_arriver(lds, 1)) {
    f32x4 ml[4];
#pragma unroll
    for (int i = 0; i < 4; ++i) ml[i] = *reinterpret_cast<const f32x4*>(&lds[L_ML + 4 * i]);
    const float mwg = fmaxf(fmaxf(fmaxf(ml[0][0], ml[0][2]), fmaxf(ml[1][0], ml[1][2])), fmaxf(fmaxf(ml[2][0], ml[2][2]), fmaxf(ml[3][0], ml[3][2])));
    float lwg = 0.f;
#pragma unroll
    for (int i = 0; i < 4; ++i) lwg += ml[i][1] * __builtin_amdgcn_exp2f(ml[i][0] - mwg) + ml[i][3] * __builtin_amdgcn_exp2f(ml[i][2] - mwg);
    if (lane == 0) PUBLISH(mwg, lwg, ml_so + sp * 128);
  }
  u32x4 wb[8];
#pragma unroll
  for (int i = 0; i < 8; ++i)
#pragma unroll
    for (int jj = 0; jj < 4; ++jj) wb[i][jj] = (i & 1) ? perm_hi(wo[2 * jj][i >> 1], wo[2 * jj + 1][i >> 1]) : perm_lo(wo[2 * jj][i >> 1], wo[2 * jj + 1][i >> 1]);
#pragma unroll
  for (int i = 0; i < 8; ++i) asm volatile("" : "+v"(wb[i]));
  SB();

  f32x2 acc2[4];
#pragma unroll
  for (int i = 0; i < 4; ++i) acc2[i] = f32x2{0.f, 0.f};
  u32x4 mlp;
  const int ml_vo = (lane & 1) * 128;
#define VSTEP(T, LOAD)                                                                  \
  {                                                                                     \
    if (((T) & 3) == 0) p4 = *reinterpret_cast<const f32x4*>(scw + RING * it + (T));    \
    _Pragma("unroll") for (int d = 0; d < 4; ++d)                                       \
      acc2[d] = __builtin_elementwise_fma(f32x2{bf_lo(ring[T][d]), bf_hi(ring[T][d])}, f32x2{p4[(T) & 3], p4[(T) & 3]}, acc2[d]); \
    asm volatile("" : "+v"(acc2[0]), "+v"(acc2[1]), "+v"(acc2[2]), "+v"(acc2[3]));     \
    SB();                                                                               \
    if (LOAD) { wait_lvl<((T) < 3) ? 1 : 0>(lvl); ring[T] = BLD(vr, kv_vo, SOFF(RING * it + RING + (T)), AUX_NT); } \
    SB();                                                                               \
    if (LOAD && ((T) & 3) == 3 && (T) != 15) RELAG((T) + 1)                             \
  }
#define VBODY(LOAD) { f32x4 p4;                                                         \
    VSTEP(0, LOAD) VSTEP(1, LOAD) VSTEP(2, LOAD) VSTEP(3, LOAD) VSTEP(4, LOAD) VSTEP(5, LOAD) VSTEP(6, LOAD) VSTEP(7, LOAD) \
    VSTEP(8, LOAD) VSTEP(9, LOAD) VSTEP(10, LOAD) VSTEP(11, LOAD) VSTEP(12, LOAD) VSTEP(13, LOAD) VSTEP(14, LOAD) VSTEP(15, LOAD) }
#pragma unroll 1
  for (int it = 0; it < NBODY - 1; ++it) {
    mlp = BLD(wsr, it == NBODY - 2 ? ml_vo : OOB, ml_so, AUX_SC1);
    SB();
    RELAG(0)
    VBODY(true)
    t_ref += (RING * TAU) >> 8;
  }
  if (!__all(TAG_OK(mlp))) {
#pragma unroll 1
    for (unsigned spins = 0; spins < SPIN_CAP; ++spins) {
      asm volatile("" ::: "memory");
      mlp = BLD(wsr, ml_vo, ml_so, AUX_SC1);
      if (__all(TAG_OK(mlp))) break;
      __builtin_amdgcn_s_sleep(1);
    }
  }
  float scale;
  {
    const float m1 = __uint_as_float(mlp[0]), l1 = __uint_as_float(mlp[2]);
    const float M = fmaxf(m1, dpp<XOR1>(m1));
    const float t = l1 * __builtin_amdgcn_exp2f(m1 - M);
    scale = __builtin_amdgcn_exp2f(mx - M) / (t + dpp<XOR1>(t));
  }
  int oa_off = ((g & 1) == ((c >> 2) & 1)) ? L_OB + w * 16 + (c >> 3) * 8 + 4 * (g >> 1) : L_ZERO;
  asm volatile("" : "+v"(scale), "+v"(oa_off));
  SB();
  { const int it = NBODY - 1; VBODY(false) }
#undef VBODY
#undef VSTEP
  WAVE_FENCE();
  {
    const float f0 = fold32(acc2[0][0], acc2[2][0]), f1 = fold32(acc2[0][1], acc2[2][1]), f2 = fold32(acc2[1][0], acc2[3][0]), f3 = fold32(acc2[1][1], acc2[3][1]);
    const float r0 = fold16(f0, f2), r1 = fold16(f1, f3);
    *reinterpret_cast<f32x2*>(&lds[L_SC + w * 256 + 8 * c + 2 * (g & 1) + 4 * (g >> 1)]) = f32x2{r0 * scale, r1 * scale};
    typedef __attribute__((address_space(3))) float lds_f;
    const unsigned cnt_a = (unsigned)(size_t)(lds_f*)&lds[L_CNT + 2], rd_a = (unsigned)(size_t)(lds_f*)&lds[L_SC + 2 * lane];
    unsigned old;
    unsigned long long sv;
    f32x4 s0, s1, s2, s3;
    asm volatile(
        "s_mov_b64 %[sv], exec\n\ts_mov_b64 exec, 1\n\t"
        "ds_add_rtn_u32 %[old], %[ca], %[one]\n\t"
        "s_mov_b64 exec, %[sv]\n\t"
        "ds_read2st64_b64 %[s0], %[ra] offset1:2\n\t"
        "ds_read2st64_b64 %[s1], %[ra] offset0:4 offset1:6\n\t"
        "ds_read2st64_b64 %[s2], %[ra] offset0:8 offset1:10\n\t"
        "ds_read2st64_b64 %[s3], %[ra] offset0:12 offset1:14\n\t"
        "s_waitcnt lgkmcnt(0)"
        : [sv] "=&s"(sv), [old] "=&v"(old), [s0] "=&v"(s0), [s1] "=&v"(s1), [s2] "=&v"(s2), [s3] "=&v"(s3)
        : [ca] "v"(cnt_a), [one] "v"(1u), [ra] "v"(rd_a)
        : "memory");
    if (__builtin_amdgcn_readfirstlane(old) == NWAVE - 1) {
      const f32x4 u = (s0 + s1) + (s2 + s3);
      PUBLISH(u[0] + u[2], u[1] + u[3], o_so + mem * 1024 + lane * 16);
    }
  }
  {
    const int o_vo = ((lane >> 3) & 3) * 1024 + (8 * w + (lane & 7)) * 16;
    u32x4 og;
#pragma unroll 1
    for (unsigned spins = 0; spins < SPIN_CAP; ++spins) {
      asm volatile("" ::: "memory");
      og = BLD(wsr, o_vo, o_so, AUX_SC1);
      if (__all(TAG_OK(og))) break;
    }
    float o0 = __uint_as_float(og[0]), o1 = __uint_as_float(og[2]);
    o0 += dpp<ROR8>(o0);
    o1 += dpp<ROR8>(o1);
    reinterpret_cast<unsigned*>(lds + L_OB)[w * 16 + (g & 1) * 8 + (lane & 7)] = cvtpk(o0, o1);
  }
  WAVE_FENCE();
  {
    const u32x4 oa = *reinterpret_cast<const u32x4*>(lds + oa_off);
    float y[8];
#pragma unroll
    for (int i = 0; i < 8; ++i)
      y[i] = __builtin_amdgcn_mfma_f32_16x16x32_bf16(__builtin_bit_cast(bf16x8, oa), __builtin_bit_cast(bf16x8, wb[i]), f32x4{0.f, 0.f, 0.f, 0.f}, 0, 0, 0)[0];
    if ((lane & 31) < 20) {
      float* yp = &lds[L_Y + w * 320 + (lane >> 5) * 160 + 8 * (lane & 31)];
      *reinterpret_cast<f32x4*>(yp) = f32x4{y[0], y[1], y[2], y[3]};
      *reinterpret_cast<f32x4*>(yp + 4) = f32x4{y[4], y[5], y[6], y[7]};
    }
  }
  __syncthreads();
  if (tid < 160) {
    f32x2 s[8];
#pragma unroll
    for (int ww = 0; ww < 8; ++ww) s[ww] = *reinterpret_cast<const f32x2*>(&lds[L_Y + ww * 320 + 2 * tid]);
    const f32x2 t = ((s[0] + s[1]) + (s[2] + s[3])) + ((s[4] + s[5]) + (s[6] + s[7]));
    PUBLISH(t[0], t[1], y_so + (h * 4 + mem) * 2560 + tid * 16);
  }
  const int unit = wgi & 127;
  if (w == 3 && unit < 80) {
    const int y_vo = (lane >> 3) * (4 * 2560) + unit * 128 + (lane & 7) * 16;
    int out_vo = lane < 8 ? (((unit / 10) & 1) * (2 * DM) + br * DM + (unit / 20) * 160 + (unit % 10) * 16 + 2 * lane) * 2 : OOB;
    asm volatile("" : "+v"(out_vo));
    __builtin_amdgcn_s_sleep(Y_DELAY);
    u32x4 yg[4];
#pragma unroll 1
    for (unsigned spins = 0; spins < SPIN_CAP; ++spins) {
      asm volatile("" ::: "memory");
#pragma unroll
      for (int i = 0; i < 4; ++i) yg[i] = BLD(wsr, y_vo + i * (8 * 4 * 2560), y_so, AUX_SC1);
      unsigned bad = 0;
#pragma unroll
      for (int i = 0; i < 4; ++i) bad |= (yg[i][1] ^ tagA) | (yg[i][3] ^ tagB);
      if (__all(bad == 0)) break;
    }
    const float s0 = (__uint_as_float(yg[0][0]) + __uint_as_float(yg[1][0])) + (__uint_as_float(yg[2][0]) + __uint_as_float(yg[3][0]));
    const float s1 = (__uint_as_float(yg[0][2]) + __uint_as_float(yg[1][2])) + (__uint_as_float(yg[2][2]) + __uint_as_float(yg[3][2]));
    float s = fold32(s0, s1);
    s = fold16(s, s);
    s += dpp<ROR8>(s);
    auto t = __builtin_amdgcn_permlane32_swap(__float_as_uint(s), __float_as_uint(s), false, false);
    __builtin_amdgcn_raw_buffer_store_b32(cvtpk(__uint_as_float(t[0]), __uint_as_float(t[1])), RSRC(pout, NB * 2 * DM * 2), out_vo, 0, 0);
    if (unit == 0 && lane == 0) __builtin_amdgcn_raw_buffer_store_b128(u32x4{e0 + 1u, e1 + 1u, e2 + 1u, e3 + 1u}, wsr, WS_EPOCH + br * 256, 0, AUX_SC1);
  }
}

}

extern "C" void kernel_launch(void* const* d_in, const int* in_sizes, int n_in, void* d_out, int out_size, void* d_ws,
                              size_t ws_size, hipStream_t stream) {
  Args a;
  for (int br = 0; br < 2; ++br) {
    a.x[br] = (const char*)d_in[5 * br + 0];
    a.k[br] = (const char*)d_in[5 * br + 1];
    a.v[br] = (const char*)d_in[5 * br + 2];
    a.wq[br] = (const char*)d_in[5 * br + 3];
    a.wo[br] = (const char*)d_in[5 * br + 4];
  }
  a.ws = (char*)d_ws;
  a.out = (unsigned short*)d_out;
  hipLaunchKernelGGL(attn_fused, dim3(NWG), dim3(NTHR), 0, stream, a);
}
```

```cpp
#include <hip/hip_runtime.h>
#include <hip/hip_bf16.h>
#include <stdint.h>

namespace {

constexpr int DM = 640, NH = 32, SK = 4096, DH = 128, NB = 2;
constexpr int NSPLIT = 2;
constexpr int NWAVE = 8, NTHR = NWAVE * 64;
constexpr int KEYS_WG = SK / NSPLIT;
constexpr int NSTEP = KEYS_WG / (4 * NWAVE);
constexpr int STEP_B = NWAVE * 1024;
constexpr int RING = 16;
constexpr int NBODY = NSTEP / RING;
constexpr int FLIGHT = 4;
constexpr int NWG = 2 * NB * NH * NSPLIT;
constexpr int AUX_NT = 2, AUX_SC1 = 16;
constexpr unsigned SPIN_CAP = 1u << 16;
constexpr int OOB = 0x40000000;
constexpr int TAU = 6930;
constexpr int T_OFF = 140;
constexpr int DSHIFT = 6;
constexpr int LEAD = -3;
constexpr int Y_DELAY = 25;

constexpr int WS_EPOCH = 0;
constexpr int WS_Q = 4096;
constexpr int WS_ML = WS_Q + 64 * 4 * 64 * 16;
constexpr int WS_O = WS_ML + 256 * 128;
constexpr int WS_Y = WS_O + 64 * 4 * 64 * 16;
constexpr int WS_END = WS_Y + 2 * 32 * 4 * 2 * 80 * 16;

typedef unsigned u32x4 __attribute__((ext_vector_type(4)));
typedef unsigned u32x3 __attribute__((ext_vector_type(3)));
typedef float f32x4 __attribute__((ext_vector_type(4)));
typedef float f32x2 __attribute__((ext_vector_type(2)));
typedef __bf16 bf16x2 __attribute__((ext_vector_type(2)));
typedef __bf16 bf16x8 __attribute__((ext_vector_type(8)));

__device__ __forceinline__ float dot2(unsigned a, unsigned b, float acc) {
  return __builtin_amdgcn_fdot2_f32_bf16(__builtin_bit_cast(bf16x2, a), __builtin_bit_cast(bf16x2, b), acc, false);
}
__device__ __forceinline__ unsigned cvtpk(float lo, float hi) {
  bf16x2 r;
  r[0] = (__bf16)lo;
  r[1] = (__bf16)hi;
  return __builtin_bit_cast(unsigned, r);
}
template <int CTRL> __device__ __forceinline__ float dpp(float x) {
  return __builtin_bit_cast(float, __builtin_amdgcn_mov_dpp(__builtin_bit_cast(int, x), CTRL, 0xf, 0xf, true));
}
constexpr int XOR1 = 0xB1, XOR2 = 0x4E, XOR7 = 0x141, ROR8 = 0x128;
__device__ __forceinline__ unsigned perm_lo(unsigned a, unsigned b) { return __builtin_amdgcn_perm(b, a, 0x05040100u); }
__device__ __forceinline__ unsigned perm_hi(unsigned a, unsigned b) { return __builtin_amdgcn_perm(b, a, 0x07060302u); }
__device__ __forceinline__ float bf_lo(unsigned u) { return __uint_as_float(u << 16); }
__device__ __forceinline__ float bf_hi(unsigned u) { return __uint_as_float(u & 0xffff0000u); }

__device__ __forceinline__ float xrow_max(float x) {
  auto s = __builtin_amdgcn_permlane16_swap(__float_as_uint(x), __float_as_uint(x), false, false);
  x = fmaxf(__uint_as_float(s[0]), __uint_as_float(s[1]));
  auto t = __builtin_amdgcn_permlane32_swap(__float_as_uint(x), __float_as_uint(x), false, false);
  return fmaxf(__uint_as_float(t[0]), __uint_as_float(t[1]));
}
__device__ __forceinline__ float fold32(float a, float b) {
  auto t = __builtin_amdgcn_permlane32_swap(__float_as_uint(a), __float_as_uint(b), false, false);
  return __uint_as_float(t[0]) + __uint_as_float(t[1]);
}
__device__ __forceinline__ float fold16(float a, float b) {
  auto t = __builtin_amdgcn_permlane16_swap(__float_as_uint(a), __float_as_uint(b), false, false);
  return __uint_as_float(t[0]) + __uint_as_float(t[1]);
}
__device__ __forceinline__ float wave_sum(float x) {
  x += dpp<XOR1>(x); x += dpp<XOR2>(x); x += dpp<XOR7>(x); x += dpp<ROR8>(x);
  x = fold16(x, x);
  return fold32(x, x);
}
template <int N> __device__ __forceinline__ void wait_vm() {
  static_assert(N >= 0 && N < 64, "");
  __builtin_amdgcn_s_waitcnt((N & 15) | (7 << 4) | (15 << 8) | ((N >> 4) << 14));
}
template <int X> __device__ __forceinline__ void wait_lvl(int lvl) {
  asm volatile(
      "s_waitcnt vmcnt(%2+5)\n\t"
      "s_cmp_gt_i32 %0, 2\n\ts_cbranch_scc1 .Lw%=\n\ts_waitcnt vmcnt(%2+4)\n\t"
      "s_cmp_gt_i32 %0, 1\n\ts_cbranch_scc1 .Lw%=\n\ts_waitcnt vmcnt(%2+3)\n\t"
      "s_cmp_gt_i32 %0, 0\n\ts_cbranch_scc1 .Lw%=\n\ts_waitcnt vmcnt(%2+2)\n\t"
      "s_cmp_gt_i32 %0, %1\n\ts_cbranch_scc1 .Lw%=\n\ts_waitcnt vmcnt(%2+1)\n"
      ".Lw%=:"
      :: "s"(lvl), "i"(LEAD), "i"(X) : "scc", "memory");
}
__device__ __forceinline__ unsigned now_ticks() { return (unsigned)__builtin_amdgcn_s_memrealtime(); }

struct Args {
  const char* x[2];
  const char* k[2];
  const char* v[2];
  const char* wq[2];
  const char* wo[2];
  char* ws;
  unsigned short* out;
};

#define RSRC(p, n) __builtin_amdgcn_make_buffer_rsrc((void*)(p), 0, (n), 0x00020000)
#define BLD(r, vo, so, aux) __builtin_amdgcn_raw_buffer_load_b128((r), (vo), (so), (aux))
#define PUBLISH(v0, v1, off) __builtin_amdgcn_raw_buffer_store_b128(u32x4{__float_as_uint(v0), tagA, __float_as_uint(v1), tagB}, wsr, (off), 0, AUX_SC1)
#define TAG_OK(v) ((v)[1] == tagA && (v)[3] == tagB)
#define SB() __builtin_amdgcn_sched_barrier(0)
#define WAVE_FENCE() __builtin_amdgcn_fence(__ATOMIC_ACQ_REL, "wavefront")

constexpr int L_SC = 0;
constexpr int L_Y = 2048;
constexpr int L_ML = L_Y + 8 * 320;
constexpr int L_OB = L_ML + 16;
constexpr int L_CNT = L_OB + 8 * 16;
constexpr int L_ZERO = L_CNT + 4;
constexpr int L_TOT = 21 * 1024;
static_assert(L_ZERO + 4 <= L_TOT && L_ZERO % 4 == 0, "");

__device__ __forceinline__ bool last_arriver(float* lds, int i) {
  unsigned old = 0;
  if ((threadIdx.x & 63) == 0) old = __hip_atomic_fetch_add(reinterpret_cast<unsigned*>(lds + L_CNT) + i, 1u, __ATOMIC_ACQ_REL, __HIP_MEMORY_SCOPE_WORKGROUP);
  return __builtin_amdgcn_readfirstlane(old) == NWAVE - 1;
}

__global__ void __launch_bounds__(NTHR, 2) attn_fused(Args a) {
  __shared__ __attribute__((aligned(16))) float lds[L_TOT];
  const int tid = threadIdx.x, lane = tid & 63, w = __builtin_amdgcn_readfirstlane(tid >> 6);
  const int g = lane >> 4, c = lane & 15;
  const unsigned t_start = now_ticks();
  if (tid < 8) reinterpret_cast<unsigned*>(lds + L_CNT)[tid] = 0u;
  const char *px0 = a.x[0], *px1 = a.x[1], *pk0 = a.k[0], *pk1 = a.k[1], *pv0 = a.v[0], *pv1 = a.v[1], *pq0 = a.wq[0], *pq1 = a.wq[1], *po0 = a.wo[0], *po1 = a.wo[1];
  char* pws = a.ws;
  unsigned short* pout = a.out;
  asm volatile("" : "+s"(px0), "+s"(px1), "+s"(pk0), "+s"(pk1), "+s"(pv0), "+s"(pv1), "+s"(pq0), "+s"(pq1), "+s"(po0), "+s"(po1), "+s"(pws), "+s"(pout));
  const int bid = blockIdx.x, xcd = bid & 7, idx = bid >> 3;
  const int grp = xcd * 8 + (idx >> 2), mem = idx & 3;
  const int br = grp >> 5, h = grp & 31, b = mem >> 1, sp = mem & 1;
  const int wgi = grp * 4 + mem;
  const int rot = wgi & (NSTEP - 1);
  const auto wsr = RSRC(pws, WS_END);
  int q_so = WS_Q + grp * 4096, ml_so = WS_ML + (wgi & ~1) * 128, o_so = WS_O + grp * 4096, y_so = WS_Y + br * (32 * 4 * 2 * 80 * 16);
  asm volatile("" : "+s"(q_so), "+s"(ml_so), "+s"(o_so), "+s"(y_so));

  const u32x4 epoch_v = BLD(wsr, 0, WS_EPOCH + br * 256, AUX_SC1 | AUX_NT);
  const int row0 = 320 * b + 40 * w + 5 * (lane >> 3);
  u32x3 xr[2];
  {
    const auto xrs = RSRC(br ? px1 : px0, NB * DM * 2);
#pragma unroll
    for (int bb = 0; bb < 2; ++bb) xr[bb] = __builtin_amdgcn_raw_buffer_load_b96(xrs, (row0 * 2) & ~3, bb * DM * 2, AUX_NT);
  }
  const auto wqr = RSRC(br ? pq1 : pq0, DM * NH * DH * 2);
  u32x4 wq[5];
#pragma unroll
  for (int j = 0; j < 5; ++j) wq[j] = BLD(wqr, row0 * (NH * DH * 2) + 16 * (lane & 7), (h * DH + sp * 64) * 2 + j * (NH * DH * 2), AUX_NT);
  SB();
  __syncthreads();
  SB();
  const size_t kvoff = ((size_t)(b * NH + h) * SK + sp * KEYS_WG) * (DH * 2);
  const auto kr = RSRC((br ? pk1 : pk0) + kvoff, KEYS_WG * DH * 2);
  const auto vr = RSRC((br ? pv1 : pv0) + kvoff, KEYS_WG * DH * 2);
  const int kv_vo = w * 1024 + lane * 16;
#define SOFF(s) ((((s) + rot) & (NSTEP - 1)) * STEP_B)
  u32x4 ring[RING];
#pragma unroll
  for (int t = 0; t < FLIGHT; ++t) ring[t] = BLD(kr, kv_vo, SOFF(t), AUX_NT);
  SB();
  const unsigned e0 = __builtin_amdgcn_readfirstlane(epoch_v[0]), e1 = __builtin_amdgcn_readfirstlane(epoch_v[1]),
                 e2 = __builtin_amdgcn_readfirstlane(epoch_v[2]), e3 = __builtin_amdgcn_readfirstlane(epoch_v[3]);
  const unsigned epoch = e0 + (e1 - e0) * 0x10001u + (e2 - e0) * 0x101u + (e3 - e0) * 0x1001u;
  const unsigned tagA = epoch + 1u, tagB = epoch ^ 0x9E3779B9u;

  {
    f32x2 qa[2][4];
#pragma unroll
    for (int bb = 0; bb < 2; ++bb) {
      const unsigned sh = (row0 & 1) * 16;
      const unsigned x01 = __builtin_amdgcn_alignbit(xr[bb][1], xr[bb][0], sh), x23 = __builtin_amdgcn_alignbit(xr[bb][2], xr[bb][1], sh), x4 = xr[bb][2] >> sh;
      const float xv[5] = {bf_lo(x01), bf_hi(x01), bf_lo(x23), bf_hi(x23), bf_lo(x4)};
#pragma unroll
      for (int d = 0; d < 4; ++d) {
        qa[bb][d] = f32x2{0.f, 0.f};
#pragma unroll
        for (int j = 0; j < 5; ++j) qa[bb][d] = __builtin_elementwise_fma(f32x2{bf_lo(wq[j][d]), bf_hi(wq[j][d])}, f32x2{xv[j], xv[j]}, qa[bb][d]);
      }
    }
    float f[8], e[4];
#pragma unroll
    for (int i = 0; i < 8; ++i) f[i] = fold32(qa[0][i >> 1][i & 1], qa[1][i >> 1][i & 1]);
#pragma unroll
    for (int i = 0; i < 4; ++i) { e[i] = fold16(f[i], f[i + 4]); e[i] += dpp<ROR8>(e[i]); }
    if (c < 8) *reinterpret_cast<f32x4*>(&lds[L_Y + w * 128 + (g >> 1) * 64 + 8 * c + 4 * (g & 1)]) = f32x4{e[0], e[1], e[2], e[3]};
  }
  if (last_arriver(lds, 0)) {
    f32x2 s = {0.f, 0.f};
#pragma unroll
    for (int ww = 0; ww < NWAVE; ++ww) s += *reinterpret_cast<const f32x2*>(&lds[L_Y + ww * 128 + 2 * lane]);
    PUBLISH(s[0], s[1], q_so + (((lane >> 5) * 2 + b) * 64 + sp * 32 + (lane & 31)) * 16);
  }
  const auto wor = RSRC((br ? po1 : po0) + (size_t)h * DH * DM * 2, DH * DM * 2);
  const int wo_vo = (lane & 31) < 20 ? (16 * w + 8 * (lane >> 5)) * (DM * 2) + mem * 320 + 16 * (lane & 31) : OOB;
  u32x4 wo[8];
  u32x4 qg0, qg1;
  const int q_vo = (b * 128 + lane) * 16;
#pragma unroll
  for (int t = FLIGHT; t < RING; ++t) {
    wait_vm<FLIGHT - 1>();
    ring[t] = BLD(kr, kv_vo, SOFF(t), AUX_NT);
    SB();
  }
  qg0 = BLD(wsr, q_vo, q_so, AUX_SC1); qg1 = BLD(wsr, q_vo + 1024, q_so, AUX_SC1);
#pragma unroll
  for (int j = 0; j < 4; ++j) {
    wait_vm<FLIGHT + 1>();
    wo[j] = BLD(wor, wo_vo, j * (DM * 2), AUX_NT);
    SB();
  }
  if (!__all(TAG_OK(qg0) && TAG_OK(qg1))) {
#pragma unroll 1
    for (unsigned spins = 0; spins < SPIN_CAP; ++spins) {
      asm volatile("" ::: "memory");
      qg0 = BLD(wsr, q_vo, q_so, AUX_SC1); qg1 = BLD(wsr, q_vo + 1024, q_so, AUX_SC1);
      if (__all(TAG_OK(qg0) && TAG_OK(qg1))) break;
      __builtin_amdgcn_s_sleep(1);
    }
  }
  reinterpret_cast<unsigned*>(lds + L_SC)[w * 256 + lane] = cvtpk(__uint_as_float(qg0[0]) + __uint_as_float(qg1[0]), __uint_as_float(qg0[2]) + __uint_as_float(qg1[2]));
  WAVE_FENCE();
  const u32x4 qv = *reinterpret_cast<const u32x4*>(lds + L_SC + w * 256 + 4 * c);
  WAVE_FENCE();
  __builtin_amdgcn_s_barrier();

  unsigned t_ref = t_start + T_OFF + (((2 * (5 + RING) + 5) * TAU) >> 9);
  int lvl;
#define RELAG(T) { lvl = (int)(now_ticks() - t_ref - (unsigned)(((T) * TAU) >> 8)) >> DSHIFT; asm volatile("" : "+s"(lvl)); SB(); }

  float* scw = &lds[L_SC + (w * 4 + g) * NSTEP];
  float mx = -INFINITY;
#define KSTEP(T)                                                                        \
  {                                                                                     \
    float d = dot2(ring[T][0], qv[0], 0.f);                                             \
    d = dot2(ring[T][1], qv[1], d);                                                     \
    d = dot2(ring[T][2], qv[2], d);                                                     \
    d = dot2(ring[T][3], qv[3], d);                                                     \
    x[(T) & 3] = d;                                                                     \
    asm volatile("" : "+v"(x[(T) & 3]));                                                \
    SB();                                                                               \
    wait_lvl<0>(lvl);                                                                   \
    ring[T] = BLD(nr, kv_vo, SOFF(RING * it + RING + (T)), AUX_NT);                     \
    SB();                                                                               \
    if (((T) & 3) == 3) {     \
      const float a0 = x[0] + dpp<ROR8>(x[0]), a1 = x[1] + dpp<ROR8>(x[1]), a2 = x[2] + dpp<ROR8>(x[2]), a3 = x[3] + dpp<ROR8>(x[3]); \
      const float b0 = (c & 8) ? a2 : a0, b1 = (c & 8) ? a3 : a1;                       \
      const float c0 = b0 + dpp<XOR7>(b0), c1 = b1 + dpp<XOR7>(b1);                     \
      float s = (c & 4) ? c1 : c0;                                                      \
      s += dpp<XOR2>(s);                                                                \
      s += dpp<XOR1>(s);                                                                \
      mx = fmaxf(mx, s);                                                                \
      scw[RING * it + (T) - 3 + (c >> 2)] = s;                                          \
      SB();                                                                             \
      if ((T) != 15) RELAG((T) + 1)                                                     \
    }                                                                                   \
  }
#pragma unroll 1
  for (int it = 0; it < NBODY; ++it) {
    const auto nr = (it == NBODY - 1) ? vr : kr;
    float x[4];
    RELAG(0)
    KSTEP(0) KSTEP(1) KSTEP(2) KSTEP(3) KSTEP(4) KSTEP(5) KSTEP(6) KSTEP(7)
    KSTEP(8) KSTEP(9) KSTEP(10) KSTEP(11) KSTEP(12) KSTEP(13) KSTEP(14) KSTEP(15)
    t_ref += (RING * TAU) >> 8;
  }
#undef KSTEP
  constexpr float C2 = 0.088388347648318440f * 1.4426950408889634f;
  mx = fmaxf(mx, dpp<ROR8>(mx));
  mx = fmaxf(mx, dpp<XOR7>(mx));
  mx = xrow_max(mx) * C2;
  WAVE_FENCE();
  {
    f32x4 s4 = *reinterpret_cast<const f32x4*>(scw + 4 * c);
#pragma unroll
    for (int t = 0; t < 4; ++t) s4[t] = __builtin_amdgcn_exp2f(fmaf(s4[t], C2, -mx));
    *reinterpret_cast<f32x4*>(scw + 4 * c) = s4;
    const float ls = wave_sum((s4[0] + s4[1]) + (s4[2] + s4[3]));
    if (lane == 0) *reinterpret_cast<f32x2*>(&lds[L_ML + 2 * w]) = f32x2{mx, ls};
  }
  if (last_arriver(lds, 1)) {
    f32x4 ml[4];
#pragma unroll
    for (int i = 0; i < 4; ++i) ml[i] = *reinterpret_cast<const f32x4*>(&lds[L_ML + 4 * i]);
    const float mwg = fmaxf(fmaxf(fmaxf(ml[0][0], ml[0][2]), fmaxf(ml[1][0], ml[1][2])), fmaxf(fmaxf(ml[2][0], ml[2][2]), fmaxf(ml[3][0], ml[3][2])));
    float lwg = 0.f;
#pragma unroll
    for (int i = 0; i < 4; ++i) lwg += ml[i][1] * __builtin_amdgcn_exp2f(ml[i][0] - mwg) + ml[i][3] * __builtin_amdgcn_exp2f(ml[i][2] - mwg);
    if (lane == 0) PUBLISH(mwg, lwg, ml_so + sp * 128);
  }
  u32x4 wb[8];
#define WTR(J0, J1) { _Pragma("unroll") for (int i = 0; i < 8; ++i) { _Pragma("unroll") for (int jj = (J0); jj < (J1); ++jj)                      \
      wb[i][jj] = (i & 1) ? perm_hi(wo[2 * jj][i >> 1], wo[2 * jj + 1][i >> 1]) : perm_lo(wo[2 * jj][i >> 1], wo[2 * jj + 1][i >> 1]); }  \
    _Pragma("unroll") for (int i = 0; i < 8; ++i) asm volatile("" : "+v"(wb[i]));                                                       \
    SB(); }
#pragma unroll
  for (int i = 0; i < 8; ++i) wb[i] = u32x4{0u, 0u, 0u, 0u};
  WTR(0, 2)

  f32x2 acc2[4];
#pragma unroll
  for (int i = 0; i < 4; ++i) acc2[i] = f32x2{0.f, 0.f};
  u32x4 mlp;
  const int ml_vo = (lane & 1) * 128;
#define VSTEP(T, LOAD)                                                                  \
  {                                                                                     \
    if (((T) & 3) == 0) p4 = *reinterpret_cast<const f32x4*>(scw + RING * it + (T));    \
    _Pragma("unroll") for (int d = 0; d < 4; ++d)                                       \
      acc2[d] = __builtin_elementwise_fma(f32x2{bf_lo(ring[T][d]), bf_hi(ring[T][d])}, f32x2{p4[(T) & 3], p4[(T) & 3]}, acc2[d]); \
    asm volatile("" : "+v"(acc2[0]), "+v"(acc2[1]), "+v"(acc2[2]), "+v"(acc2[3]));     \
    SB();                                                                               \
    if (LOAD) { wait_lvl<((T) < 3) ? 1 : 0>(lvl); ring[T] = BLD(vr, kv_vo, SOFF(RING * it + RING + (T)), AUX_NT); } \
    SB();                                                                               \
    if (LOAD && ((T) & 3) == 3 && (T) != 15) RELAG((T) + 1)                             \
    if (!(LOAD) && (T) < 4) { wo[(4 + (T)) & 7] = BLD(wor, wo_vo, (4 + (T)) * (DM * 2), AUX_NT); SB(); } \
  }
#define VBODY(LOAD) { f32x4 p4;                                                         \
    VSTEP(0, LOAD) VSTEP(1, LOAD) VSTEP(2, LOAD) VSTEP(3, LOAD) VSTEP(4, LOAD) VSTEP(5, LOAD) VSTEP(6, LOAD) VSTEP(7, LOAD) \
    VSTEP(8, LOAD) VSTEP(9, LOAD) VSTEP(10, LOAD) VSTEP(11, LOAD) VSTEP(12, LOAD) VSTEP(13, LOAD) VSTEP(14, LOAD) VSTEP(15, LOAD) }
#pragma unroll 1
  for (int it = 0; it < NBODY - 1; ++it) {
    mlp = BLD(wsr, it == NBODY - 2 ? ml_vo : OOB, ml_so, AUX_SC1);
    SB();
    RELAG(0)
    VBODY(true)
    t_ref += (RING * TAU) >> 8;
  }
  if (!__all(TAG_OK(mlp))) {
#pragma unroll 1
    for (unsigned spins = 0; spins < SPIN_CAP; ++spins) {
      asm volatile("" ::: "memory");
      mlp = BLD(wsr, ml_vo, ml_so, AUX_SC1);
      if (__all(TAG_OK(mlp))) break;
      __builtin_amdgcn_s_sleep(1);
    }
  }
  float scale;
  {
    const float m1 = __uint_as_float(mlp[0]), l1 = __uint_as_float(mlp[2]);
    const float M = fmaxf(m1, dpp<XOR1>(m1));
    const float t = l1 * __builtin_amdgcn_exp2f(m1 - M);
    scale = __builtin_amdgcn_exp2f(mx - M) / (t + dpp<XOR1>(t));
  }
  int oa_off = ((g & 1) == ((c >> 2) & 1)) ? L_OB + w * 16 + (c >> 3) * 8 + 4 * (g >> 1) : L_ZERO;
  asm volatile("" : "+v"(scale), "+v"(oa_off));
  SB();
  { const int it = NBODY - 1; VBODY(false) }
#undef VBODY
#undef VSTEP
  WAVE_FENCE();
  {
    const float f0 = fold32(acc2[0][0], acc2[2][0]), f1 = fold32(acc2[0][1], acc2[2][1]), f2 = fold32(acc2[1][0], acc2[3][0]), f3 = fold32(acc2[1][1], acc2[3][1]);
    const float r0 = fold16(f0, f2), r1 = fold16(f1, f3);
    *reinterpret_cast<f32x2*>(&lds[L_SC + w * 256 + 8 * c + 2 * (g & 1) + 4 * (g >> 1)]) = f32x2{r0 * scale, r1 * scale};
    typedef __attribute__((address_space(3))) float lds_f;
    const unsigned cnt_a = (unsigned)(size_t)(lds_f*)&lds[L_CNT + 2], rd_a = (unsigned)(size_t)(lds_f*)&lds[L_SC + 2 * lane];
    unsigned old;
    unsigned long long sv;
    f32x4 s0, s1, s2, s3;
    asm volatile(
        "s_mov_b64 %[sv], exec\n\ts_mov_b64 exec, 1\n\t"
        "ds_add_rtn_u32 %[old], %[ca], %[one]\n\t"
        "s_mov_b64 exec, %[sv]\n\t"
        "ds_read2st64_b64 %[s0], %[ra] offset1:2\n\t"
        "ds_read2st64_b64 %[s1], %[ra] offset0:4 offset1:6\n\t"
        "ds_read2st64_b64 %[s2], %[ra] offset0:8 offset1:10\n\t"
        "ds_read2st64_b64 %[s3], %[ra] offset0:12 offset1:14\n\t"
        "s_waitcnt lgkmcnt(0)"
        : [sv] "=&s"(sv), [old] "=&v"(old), [s0] "=&v"(s0), [s1] "=&v"(s1), [s2] "=&v"(s2), [s3] "=&v"(s3)
        : [ca] "v"(cnt_a), [one] "v"(1u), [ra] "v"(rd_a)
        : "memory");
    if (__builtin_amdgcn_readfirstlane(old) == NWAVE - 1) {
      const f32x4 u = (s0 + s1) + (s2 + s3);
      PUBLISH(u[0] + u[2], u[1] + u[3], o_so + mem * 1024 + lane * 16);
    }
  }
  {
    const int o_vo = ((lane >> 3) & 3) * 1024 + (8 * w + (lane & 7)) * 16;
    SB();
    WTR(2, 3)
    u32x4 og = BLD(wsr, o_vo, o_so, AUX_SC1);
    SB();
    WTR(3, 4)
    if (!__all(TAG_OK(og))) {
#pragma unroll 1
      for (unsigned spins = 0; spins < SPIN_CAP; ++spins) {
        asm volatile("" ::: "memory");
        og = BLD(wsr, o_vo, o_so, AUX_SC1);
        if (__all(TAG_OK(og))) break;
      }
    }
    float o0 = __uint_as_float(og[0]), o1 = __uint_as_float(og[2]);
    o0 += dpp<ROR8>(o0);
    o1 += dpp<ROR8>(o1);
    reinterpret_cast<unsigned*>(lds + L_OB)[w * 16 + (g & 1) * 8 + (lane & 7)] = cvtpk(o0, o1);
  }
  WAVE_FENCE();
  {
    const u32x4 oa = *reinterpret_cast<const u32x4*>(lds + oa_off);
    float y[8];
#pragma unroll
    for (int i = 0; i < 8; ++i)
      y[i] = __builtin_amdgcn_mfma_f32_16x16x32_bf16(__builtin_bit_cast(bf16x8, oa), __builtin_bit_cast(bf16x8, wb[i]), f32x4{0.f, 0.f, 0.f, 0.f}, 0, 0, 0)[0];
    if ((lane & 31) < 20) {
      float* yp = &lds[L_Y + w * 320 + (lane >> 5) * 160 + 8 * (lane & 31)];
      *reinterpret_cast<f32x4*>(yp) = f32x4{y[0], y[1], y[2], y[3]};
      *reinterpret_cast<f32x4*>(yp + 4) = f32x4{y[4], y[5], y[6], y[7]};
    }
  }
  __syncthreads();
  if (tid < 160) {
    f32x2 s[8];
#pragma unroll
    for (int ww = 0; ww < 8; ++ww) s[ww] = *reinterpret_cast<const f32x2*>(&lds[L_Y + ww * 320 + 2 * tid]);
    const f32x2 t = ((s[0] + s[1]) + (s[2] + s[3])) + ((s[4] + s[5]) + (s[6] + s[7]));
    PUBLISH(t[0], t[1], y_so + (h * 4 + mem) * 2560 + tid * 16);
  }
  const int unit = wgi & 127;
  if (w == 3 && unit < 80) {
    const int y_vo = (lane >> 3) * (4 * 2560) + unit * 128 + (lane & 7) * 16;
    int out_vo = lane < 8 ? (((unit / 10) & 1) * (2 * DM) + br * DM + (unit / 20) * 160 + (unit % 10) * 16 + 2 * lane) * 2 : OOB;
    asm volatile("" : "+v"(out_vo));
    __builtin_amdgcn_s_sleep(Y_DELAY);
    u32x4 yg[4];
#pragma unroll 1
    for (unsigned spins = 0; spins < SPIN_CAP; ++spins) {
      asm volatile("" ::: "memory");
#pragma unroll
      for (int i = 0; i < 4; ++i) yg[i] = BLD(wsr, y_vo + i * (8 * 4 * 2560), y_so, AUX_SC1);
      unsigned bad = 0;
#pragma unroll
      for (int i = 0; i < 4; ++i) bad |= (yg[i][1] ^ tagA) | (yg[i][3] ^ tagB);
      if (__all(bad == 0)) break;
    }
    const float s0 = (__uint_as_float(yg[0][0]) + __uint_as_float(yg[1][0])) + (__uint_as_float(yg[2][0]) + __uint_as_float(yg[3][0]));
    const float s1 = (__uint_as_float(yg[0][2]) + __uint_as_float(yg[1][2])) + (__uint_as_float(yg[2][2]) + __uint_as_float(yg[3][2]));
    float s = fold32(s0, s1);
    s = fold16(s, s);
    s += dpp<ROR8>(s);
    auto t = __builtin_amdgcn_permlane32_swap(__float_as_uint(s), __float_as_uint(s), false, false);
    __builtin_amdgcn_raw_buffer_store_b32(cvtpk(__uint_as_float(t[0]), __uint_as_float(t[1])), RSRC(pout, NB * 2 * DM * 2), out_vo, 0, 0);
    if (unit == 0 && lane == 0) __builtin_amdgcn_raw_buffer_store_b128(u32x4{e0 + 1u, e1 + 1u, e2 + 1u, e3 + 1u}, wsr, WS_EPOCH + br * 256, 0, AUX_SC1);
  }
}

}

extern "C" void kernel_launch(void* const* d_in, const int* in_sizes, int n_in, void* d_out, int out_size, void* d_ws,
                              size_t ws_size, hipStream_t stream) {
  Args a;
  for (int br = 0; br < 2; ++br) {
    a.x[br] = (const char*)d_in[5 * br + 0];
    a.k[br] = (const char*)d_in[5 * br + 1];
    a.v[br] = (const char*)d_in[5 * br + 2];
    a.wq[br] = (const char*)d_in[5 * br + 3];
    a.wo[br] = (const char*)d_in[5 * br + 4];
  }
  a.ws = (char*)d_ws;
  a.out = (unsigned short*)d_out;
  hipLaunchKernelGGL(attn_fused, dim3(NWG), dim3(NTHR), 0, stream, a);
}
```

```cpp
#include <hip/hip_runtime.h>
#include <hip/hip_bf16.h>
#include <stdint.h>

#define STAMPS 0
#define STAMPW 0
#define LEAD_ -3
#define WL 8
#define PJ 3
#define WLAT 0
#define TAU_ 6930
#define TOFF_ 140
#define YD_ 25

namespace {

constexpr int DM = 640, NH = 32, SK = 4096, DH = 128, NB = 2;
constexpr int NSPLIT = 2;
constexpr int NWAVE = 8, NTHR = NWAVE * 64;
constexpr int KEYS_WG = SK / NSPLIT;
constexpr int NSTEP = KEYS_WG / (4 * NWAVE);
constexpr int STEP_B = NWAVE * 1024;
constexpr int RING = 16;
constexpr int NBODY = NSTEP / RING;
constexpr int FLIGHT = 4;
constexpr int NWG = 2 * NB * NH * NSPLIT;
constexpr int AUX_NT = 2, AUX_SC1 = 16;
constexpr unsigned SPIN_CAP = 1u << 16;
constexpr int OOB = 0x40000000;
constexpr int TAU = TAU_;
constexpr int T_OFF = TOFF_;
constexpr int DSHIFT = 6;
constexpr int LEAD = LEAD_;
constexpr int Y_DELAY = YD_;

constexpr int WS_EPOCH = 0;
constexpr int WS_Q = 4096;
constexpr int WS_ML = WS_Q + 64 * 4 * 64 * 16;
constexpr int WS_O = WS_ML + 256 * 128;
constexpr int WS_Y = WS_O + 64 * 4 * 64 * 16;
constexpr int WS_END = WS_Y + 2 * 32 * 4 * 2 * 80 * 16;

typedef unsigned u32x4 __attribute__((ext_vector_type(4)));
typedef unsigned u32x3 __attribute__((ext_vector_type(3)));
typedef float f32x4 __attribute__((ext_vector_type(4)));
typedef float f32x2 __attribute__((ext_vector_type(2)));
typedef __bf16 bf16x2 __attribute__((ext_vector_type(2)));
typedef __bf16 bf16x8 __attribute__((ext_vector_type(8)));

__device__ __forceinline__ float dot2(unsigned a, unsigned b, float acc) {
  return __builtin_amdgcn_fdot2_f32_bf16(__builtin_bit_cast(bf16x2, a), __builtin_bit_cast(bf16x2, b), acc, false);
}
__device__ __forceinline__ unsigned cvtpk(float lo, float hi) {
  bf16x2 r;
  r[0] = (__bf16)lo;
  r[1] = (__bf16)hi;
  return __builtin_bit_cast(unsigned, r);
}
template <int CTRL> __device__ __forceinline__ float dpp(float x) {
  return __builtin_bit_cast(float, __builtin_amdgcn_mov_dpp(__builtin_bit_cast(int, x), CTRL, 0xf, 0xf, true));
}
constexpr int XOR1 = 0xB1, XOR2 = 0x4E, XOR7 = 0x141, ROR8 = 0x128;
__device__ __forceinline__ unsigned perm_lo(unsigned a, unsigned b) { return __builtin_amdgcn_perm(b, a, 0x05040100u); }
__device__ __forceinline__ unsigned perm_hi(unsigned a, unsigned b) { return __builtin_amdgcn_perm(b, a, 0x07060302u); }
__device__ __forceinline__ float bf_lo(unsigned u) { return __uint_as_float(u << 16); }
__device__ __forceinline__ float bf_hi(unsigned u) { return __uint_as_float(u & 0xffff0000u); }

__device__ __forceinline__ float xrow_max(float x) {
  auto s = __builtin_amdgcn_permlane16_swap(__float_as_uint(x), __float_as_uint(x), false, false);
  x = fmaxf(__uint_as_float(s[0]), __uint_as_float(s[1]));
  auto t = __builtin_amdgcn_permlane32_swap(__float_as_uint(x), __float_as_uint(x), false, false);
  return fmaxf(__uint_as_float(t[0]), __uint_as_float(t[1]));
}
__device__ __forceinline__ float fold32(float a, float b) {
  auto t = __builtin_amdgcn_permlane32_swap(__float_as_uint(a), __float_as_uint(b), false, false);
  return __uint_as_float(t[0]) + __uint_as_float(t[1]);
}
__device__ __forceinline__ float fold16(float a, float b) {
  auto t = __builtin_amdgcn_permlane16_swap(__float_as_uint(a), __float_as_uint(b), false, false);
  return __uint_as_float(t[0]) + __uint_as_float(t[1]);
}
__device__ __forceinline__ float wave_sum(float x) {
  x += dpp<XOR1>(x); x += dpp<XOR2>(x); x += dpp<XOR7>(x); x += dpp<ROR8>(x);
  x = fold16(x, x);
  return fold32(x, x);
}
template <int N> __device__ __forceinline__ void wait_vm() {
  static_assert(N >= 0 && N < 64, "");
  __builtin_amdgcn_s_waitcnt((N & 15) | (7 << 4) | (15 << 8) | ((N >> 4) << 14));
}
template <int X> __device__ __forceinline__ void wait_lvl(int lvl) {
  asm volatile(
      "s_waitcnt vmcnt(%2+5)\n\t"
      "s_cmp_gt_i32 %0, 2\n\ts_cbranch_scc1 .Lw%=\n\ts_waitcnt vmcnt(%2+4)\n\t"
      "s_cmp_gt_i32 %0, 1\n\ts_cbranch_scc1 .Lw%=\n\ts_waitcnt vmcnt(%2+3)\n\t"
      "s_cmp_gt_i32 %0, 0\n\ts_cbranch_scc1 .Lw%=\n\ts_waitcnt vmcnt(%2+2)\n\t"
      "s_cmp_gt_i32 %0, %1\n\ts_cbranch_scc1 .Lw%=\n\ts_waitcnt vmcnt(%2+1)\n"
      ".Lw%=:"
      :: "s"(lvl), "i"(LEAD), "i"(X) : "scc", "memory");
}
__device__ __forceinline__ unsigned now_ticks() { return (unsigned)__builtin_amdgcn_s_memrealtime(); }

struct Args {
  const char* x[2];
  const char* k[2];
  const char* v[2];
  const char* wq[2];
  const char* wo[2];
  char* ws;
  unsigned short* out;
};

#define RSRC(p, n) __builtin_amdgcn_make_buffer_rsrc((void*)(p), 0, (n), 0x00020000)
#define BLD(r, vo, so, aux) __builtin_amdgcn_raw_buffer_load_b128((r), (vo), (so), (aux))
#define PUBLISH(v0, v1, off) __builtin_amdgcn_raw_buffer_store_b128(u32x4{__float_as_uint(v0), tagA, __float_as_uint(v1), tagB}, wsr, (off), 0, AUX_SC1)
#define TAG_OK(v) ((v)[1] == tagA && (v)[3] == tagB)
#define SB() __builtin_amdgcn_sched_barrier(0)
#define WAVE_FENCE() __builtin_amdgcn_fence(__ATOMIC_ACQ_REL, "wavefront")
#define STAMP(i)

constexpr int L_SC = 0;
constexpr int L_Y = 2048;
constexpr int L_ML = L_Y + 8 * 320;
constexpr int L_OB = L_ML + 16;
constexpr int L_CNT = L_OB + 8 * 16;
constexpr int L_ZERO = L_CNT + 4;
constexpr int L_TOT = 21 * 1024;
static_assert(L_ZERO + 4 <= L_TOT && L_ZERO % 4 == 0, "");

__device__ __forceinline__ bool last_arriver(float* lds, int i) {
  unsigned old = 0;
  if ((threadIdx.x & 63) == 0) old = __hip_atomic_fetch_add(reinterpret_cast<unsigned*>(lds + L_CNT) + i, 1u, __ATOMIC_ACQ_REL, __HIP_MEMORY_SCOPE_WORKGROUP);
  return __builtin_amdgcn_readfirstlane(old) == NWAVE - 1;
}

__global__ void __launch_bounds__(NTHR, 2) attn_fused(Args a) {
  __shared__ __attribute__((aligned(16))) float lds[L_TOT];
  const int tid = threadIdx.x, lane = tid & 63, w = __builtin_amdgcn_readfirstlane(tid >> 6);
  const int g = lane >> 4, c = lane & 15;
  STAMP(0);
  const unsigned t_start = now_ticks();
  if (tid < 8) reinterpret_cast<unsigned*>(lds + L_CNT)[tid] = 0u;
  const char *px0 = a.x[0], *px1 = a.x[1], *pk0 = a.k[0], *pk1 = a.k[1], *pv0 = a.v[0], *pv1 = a.v[1], *pq0 = a.wq[0], *pq1 = a.wq[1], *po0 = a.wo[0], *po1 = a.wo[1];
  char* pws = a.ws;
  unsigned short* pout = a.out;
  asm volatile("" : "+s"(px0), "+s"(px1), "+s"(pk0), "+s"(pk1), "+s"(pv0), "+s"(pv1), "+s"(pq0), "+s"(pq1), "+s"(po0), "+s"(po1), "+s"(pws), "+s"(pout));
  const int bid = blockIdx.x, xcd = bid & 7, idx = bid >> 3;
  const int grp = xcd * 8 + (idx >> 2), mem = idx & 3;
  const int br = grp >> 5, h = grp & 31, b = mem >> 1, sp = mem & 1;
  const int wgi = grp * 4 + mem;
  const int rot = wgi & (NSTEP - 1);
  const auto wsr = RSRC(pws, WS_END);
  int q_so = WS_Q + grp * 4096, ml_so = WS_ML + (wgi & ~1) * 128, o_so = WS_O + grp * 4096, y_so = WS_Y + br * (32 * 4 * 2 * 80 * 16);
  asm volatile("" : "+s"(q_so), "+s"(ml_so), "+s"(o_so), "+s"(y_so));

  const u32x4 epoch_v = BLD(wsr, 0, WS_EPOCH + br * 256, AUX_SC1 | AUX_NT);
  const int row0 = 320 * b + 40 * w + 5 * (lane >> 3);
  u32x3 xr[2];
  {
    const auto xrs = RSRC(br ? px1 : px0, NB * DM * 2);
#pragma unroll
    for (int bb = 0; bb < 2; ++bb) xr[bb] = __builtin_amdgcn_raw_buffer_load_b96(xrs, (row0 * 2) & ~3, bb * DM * 2, AUX_NT);
  }
  const auto wqr = RSRC(br ? pq1 : pq0, DM * NH * DH * 2);
  u32x4 wq[5];
#pragma unroll
  for (int j = 0; j < 5; ++j) wq[j] = BLD(wqr, row0 * (NH * DH * 2) + 16 * (lane & 7), (h * DH + sp * 64) * 2 + j * (NH * DH * 2), AUX_NT);
  SB();
  __syncthreads();
  SB();
  const size_t kvoff = ((size_t)(b * NH + h) * SK + sp * KEYS_WG) * (DH * 2);
  const auto kr = RSRC((br ? pk1 : pk0) + kvoff, KEYS_WG * DH * 2);
  const auto vr = RSRC((br ? pv1 : pv0) + kvoff, KEYS_WG * DH * 2);
  const int kv_vo = w * 1024 + lane * 16;
#define SOFF(s) ((((s) + rot) & (NSTEP - 1)) * STEP_B)
  u32x4 ring[RING];
#pragma unroll
  for (int t = 0; t < FLIGHT; ++t) ring[t] = BLD(kr, kv_vo, SOFF(t), AUX_NT);
  SB();
  const unsigned e0 = __builtin_amdgcn_readfirstlane(epoch_v[0]), e1 = __builtin_amdgcn_readfirstlane(epoch_v[1]),
                 e2 = __builtin_amdgcn_readfirstlane(epoch_v[2]), e3 = __builtin_amdgcn_readfirstlane(epoch_v[3]);
  const unsigned epoch = e0 + (e1 - e0) * 0x10001u + (e2 - e0) * 0x101u + (e3 - e0) * 0x1001u;
  const unsigned tagA = epoch + 1u, tagB = epoch ^ 0x9E3779B9u;

  {
    f32x2 qa[2][4];
#pragma unroll
    for (int bb = 0; bb < 2; ++bb) {
      const unsigned sh = (row0 & 1) * 16;
      const unsigned x01 = __builtin_amdgcn_alignbit(xr[bb][1], xr[bb][0], sh), x23 = __builtin_amdgcn_alignbit(xr[bb][2], xr[bb][1], sh), x4 = xr[bb][2] >> sh;
      const float xv[5] = {bf_lo(x01), bf_hi(x01), bf_lo(x23), bf_hi(x23), bf_lo(x4)};
#pragma unroll
      for (int d = 0; d < 4; ++d) {
        qa[bb][d] = f32x2{0.f, 0.f};
#pragma unroll
        for (int j = 0; j < 5; ++j) qa[bb][d] = __builtin_elementwise_fma(f32x2{bf_lo(wq[j][d]), bf_hi(wq[j][d])}, f32x2{xv[j], xv[j]}, qa[bb][d]);
      }
    }
    float f[8], e[4];
#pragma unroll
    for (int i = 0; i < 8; ++i) f[i] = fold32(qa[0][i >> 1][i & 1], qa[1][i >> 1][i & 1]);
#pragma unroll
    for (int i = 0; i < 4; ++i) { e[i] = fold16(f[i], f[i + 4]); e[i] += dpp<ROR8>(e[i]); }
    if (c < 8) *reinterpret_cast<f32x4*>(&lds[L_Y + w * 128 + (g >> 1) * 64 + 8 * c + 4 * (g & 1)]) = f32x4{e[0], e[1], e[2], e[3]};
  }
  if (last_arriver(lds, 0)) {
    f32x2 s = {0.f, 0.f};
#pragma unroll
    for (int ww = 0; ww < NWAVE; ++ww) s += *reinterpret_cast<const f32x2*>(&lds[L_Y + ww * 128 + 2 * lane]);
    PUBLISH(s[0], s[1], q_so + (((lane >> 5) * 2 + b) * 64 + sp * 32 + (lane & 31)) * 16);
  }
  STAMP(1);
  const auto wor = RSRC((br ? po1 : po0) + (size_t)h * DH * DM * 2, DH * DM * 2);
  const int wo_vo = (lane & 31) < 20 ? (16 * w + 8 * (lane >> 5)) * (DM * 2) + mem * 320 + 16 * (lane & 31) : OOB;
  u32x4 wo[8];
  u32x4 qg0, qg1;
  const int q_vo = (b * 128 + lane) * 16;
#pragma unroll
  for (int t = FLIGHT; t < RING + 8 - WL; ++t) {
    if (t == RING + 8 - WL - FLIGHT) { qg0 = BLD(wsr, q_vo, q_so, AUX_SC1); qg1 = BLD(wsr, q_vo + 1024, q_so, AUX_SC1); }
    if (t >= RING + 8 - WL - FLIGHT) wait_vm<FLIGHT + 1>(); else wait_vm<FLIGHT - 1>();
    if (t < RING) ring[t] = BLD(kr, kv_vo, SOFF(t), AUX_NT); else wo[(t - RING) & 7] = BLD(wor, wo_vo, (t - RING) * (DM * 2), AUX_NT);
    SB();
  }
  if (!__all(TAG_OK(qg0) && TAG_OK(qg1))) {
#pragma unroll 1
    for (unsigned spins = 0; spins < SPIN_CAP; ++spins) {
      asm volatile("" ::: "memory");
      qg0 = BLD(wsr, q_vo, q_so, AUX_SC1); qg1 = BLD(wsr, q_vo + 1024, q_so, AUX_SC1);
      if (__all(TAG_OK(qg0) && TAG_OK(qg1))) break;
      __builtin_amdgcn_s_sleep(1);
    }
  }
  reinterpret_cast<unsigned*>(lds + L_SC)[w * 256 + lane] = cvtpk(__uint_as_float(qg0[0]) + __uint_as_float(qg1[0]), __uint_as_float(qg0[2]) + __uint_as_float(qg1[2]));
  WAVE_FENCE();
  const u32x4 qv = *reinterpret_cast<const u32x4*>(lds + L_SC + w * 256 + 4 * c);
  WAVE_FENCE();
  __builtin_amdgcn_s_barrier();
  STAMP(2);

  unsigned t_ref = t_start + T_OFF + ((((5 + RING) * 8 + 5 * (8 - WL)) * TAU) >> 11);
  int lvl;
#define RELAG(T) { lvl = (int)(now_ticks() - t_ref - (unsigned)(((T) * TAU) >> 8)) >> DSHIFT; asm volatile("" : "+s"(lvl)); SB(); }

  float* scw = &lds[L_SC + (w * 4 + g) * NSTEP];
  float mx = -INFINITY;
#define KSTEP(T)                                                                        \
  {                                                                                     \
    float d = dot2(ring[T][0], qv[0], 0.f);                                             \
    d = dot2(ring[T][1], qv[1], d);                                                     \
    d = dot2(ring[T][2], qv[2], d);                                                     \
    d = dot2(ring[T][3], qv[3], d);                                                     \
    x[(T) & 3] = d;                                                                     \
    asm volatile("" : "+v"(x[(T) & 3]));                                                \
    SB();                                                                               \
    wait_lvl<0>(lvl);                                                                   \
    ring[T] = BLD(nr, kv_vo, SOFF(RING * it + RING + (T)), AUX_NT);                     \
    SB();                                                                               \
    if (((T) & 3) == 3) {     \
      const float a0 = x[0] + dpp<ROR8>(x[0]), a1 = x[1] + dpp<ROR8>(x[1]), a2 = x[2] + dpp<ROR8>(x[2]), a3 = x[3] + dpp<ROR8>(x[3]); \
      const float b0 = (c & 8) ? a2 : a0, b1 = (c & 8) ? a3 : a1;                       \
      const float c0 = b0 + dpp<XOR7>(b0), c1 = b1 + dpp<XOR7>(b1);                     \
      float s = (c & 4) ? c1 : c0;                                                      \
      s += dpp<XOR2>(s);                                                                \
      s += dpp<XOR1>(s);                                                                \
      mx = fmaxf(mx, s);                                                                \
      scw[RING * it + (T) - 3 + (c >> 2)] = s;                                          \
      SB();                                                                             \
      if ((T) != 15) RELAG((T) + 1)                                                     \
    }                                                                                   \
  }
#pragma unroll 1
  for (int it = 0; it < NBODY; ++it) {
    const auto nr = (it == NBODY - 1) ? vr : kr;
    float x[4];
    RELAG(0)
    KSTEP(0) KSTEP(1) KSTEP(2) KSTEP(3) KSTEP(4) KSTEP(5) KSTEP(6) KSTEP(7)
    KSTEP(8) KSTEP(9) KSTEP(10) KSTEP(11) KSTEP(12) KSTEP(13) KSTEP(14) KSTEP(15)
    t_ref += (RING * TAU) >> 8;
  }
#undef KSTEP
  STAMP(3);
  constexpr float C2 = 0.088388347648318440f * 1.4426950408889634f;
  mx = fmaxf(mx, dpp<ROR8>(mx));
  mx = fmaxf(mx, dpp<XOR7>(mx));
  mx = xrow_max(mx) * C2;
  WAVE_FENCE();
  {
    f32x4 s4 = *reinterpret_cast<const f32x4*>(scw + 4 * c);
#pragma unroll
    for (int t = 0; t < 4; ++t) s4[t] = __builtin_amdgcn_exp2f(fmaf(s4[t], C2, -mx));
    *reinterpret_cast<f32x4*>(scw + 4 * c) = s4;
    const float ls = wave_sum((s4[0] + s4[1]) + (s4[2] + s4[3]));
    if (lane == 0) *reinterpret_cast<f32x2*>(&lds[L_ML + 2 * w]) = f32x2{mx, ls};
  }
  if (last_arriver(lds, 1)) {
    f32x4 ml[4];
#pragma unroll
    for (int i = 0; i < 4; ++i) ml[i] = *reinterpret_cast<const f32x4*>(&lds[L_ML + 4 * i]);
    const float mwg = fmaxf(fmaxf(fmaxf(ml[0][0], ml[0][2]), fmaxf(ml[1][0], ml[1][2])), fmaxf(fmaxf(ml[2][0], ml[2][2]), fmaxf(ml[3][0], ml[3][2])));
    float lwg = 0.f;
#pragma unroll
    for (int i = 0; i < 4; ++i) lwg += ml[i][1] * __builtin_amdgcn_exp2f(ml[i][0] - mwg) + ml[i][3] * __builtin_amdgcn_exp2f(ml[i][2] - mwg);
    if (lane == 0) PUBLISH(mwg, lwg, ml_so + sp * 128);
  }
  u32x4 wb[8];
#define WTR(J0, J1) _Pragma("unroll") for (int i = 0; i < 8; ++i) _Pragma("unroll") for (int jj = (J0); jj < (J1); ++jj) { \
    wb[i][jj] = (i & 1) ? perm_hi(wo[2 * jj][i >> 1], wo[2 * jj + 1][i >> 1]) : perm_lo(wo[2 * jj][i >> 1], wo[2 * jj + 1][i >> 1]); }
#pragma unroll
  for (int i = 0; i < 8; ++i) wb[i] = u32x4{0u, 0u, 0u, 0u};
  WTR(0, (8 - WL) / 2)
#pragma unroll
  for (int i = 0; i < 8; ++i) asm volatile("" : "+v"(wb[i]));
  SB();

  f32x2 acc2[4];
#pragma unroll
  for (int i = 0; i < 4; ++i) acc2[i] = f32x2{0.f, 0.f};
  u32x4 mlp;
  const int ml_vo = (lane & 1) * 128;
#define VSTEP(T, LOAD)                                                                  \
  {                                                                                     \
    if (((T) & 3) == 0) p4 = *reinterpret_cast<const f32x4*>(scw + RING * it + (T));    \
    _Pragma("unroll") for (int d = 0; d < 4; ++d)                                       \
      acc2[d] = __builtin_elementwise_fma(f32x2{bf_lo(ring[T][d]), bf_hi(ring[T][d])}, f32x2{p4[(T) & 3], p4[(T) & 3]}, acc2[d]); \
    asm volatile("" : "+v"(acc2[0]), "+v"(acc2[1]), "+v"(acc2[2]), "+v"(acc2[3]));     \
    SB();                                                                               \
    if (LOAD) { wait_lvl<((T) < 3) ? 1 : 0>(lvl); ring[T] = BLD(vr, kv_vo, SOFF(RING * it + RING + (T)), AUX_NT); } \
    SB();                                                                               \
    if (LOAD && ((T) & 3) == 3 && (T) != 15) RELAG((T) + 1)                             \
    if (WL && !(LOAD) && (T) >= WLAT && (T) < WLAT + 4) { _Pragma("unroll") for (int u = 0; u < WL / 4; ++u) { const int j_ = (8 - WL + ((T) - WLAT) * (WL / 4) + u) & 7; wo[j_] = BLD(wor, wo_vo, j_ * (DM * 2), AUX_NT); } SB(); } \
  }
#define VBODY(LOAD) { f32x4 p4;                                                         \
    VSTEP(0, LOAD) VSTEP(1, LOAD) VSTEP(2, LOAD) VSTEP(3, LOAD) VSTEP(4, LOAD) VSTEP(5, LOAD) VSTEP(6, LOAD) VSTEP(7, LOAD) \
    VSTEP(8, LOAD) VSTEP(9, LOAD) VSTEP(10, LOAD) VSTEP(11, LOAD) VSTEP(12, LOAD) VSTEP(13, LOAD) VSTEP(14, LOAD) VSTEP(15, LOAD) }
#pragma unroll 1
  for (int it = 0; it < NBODY - 1; ++it) {
    mlp = BLD(wsr, it == NBODY - 2 ? ml_vo : OOB, ml_so, AUX_SC1);
    SB();
    RELAG(0)
    VBODY(true)
    t_ref += (RING * TAU) >> 8;
  }
  if (!__all(TAG_OK(mlp))) {
#pragma unroll 1
    for (unsigned spins = 0; spins < SPIN_CAP; ++spins) {
      asm volatile("" ::: "memory");
      mlp = BLD(wsr, ml_vo, ml_so, AUX_SC1);
      if (__all(TAG_OK(mlp))) break;
      __builtin_amdgcn_s_sleep(1);
    }
  }
  float scale;
  {
    const float m1 = __uint_as_float(mlp[0]), l1 = __uint_as_float(mlp[2]);
    const float M = fmaxf(m1, dpp<XOR1>(m1));
    const float t = l1 * __builtin_amdgcn_exp2f(m1 - M);
    scale = __builtin_amdgcn_exp2f(mx - M) / (t + dpp<XOR1>(t));
  }
  int oa_off = ((g & 1) == ((c >> 2) & 1)) ? L_OB + w * 16 + (c >> 3) * 8 + 4 * (g >> 1) : L_ZERO;
  asm volatile("" : "+v"(scale), "+v"(oa_off));
  SB();
  { const int it = NBODY - 1; VBODY(false) }
#undef VBODY
#undef VSTEP
  WAVE_FENCE();
  STAMP(4);
  {
    const float f0 = fold32(acc2[0][0], acc2[2][0]), f1 = fold32(acc2[0][1], acc2[2][1]), f2 = fold32(acc2[1][0], acc2[3][0]), f3 = fold32(acc2[1][1], acc2[3][1]);
    const float r0 = fold16(f0, f2), r1 = fold16(f1, f3);
    *reinterpret_cast<f32x2*>(&lds[L_SC + w * 256 + 8 * c + 2 * (g & 1) + 4 * (g >> 1)]) = f32x2{r0 * scale, r1 * scale};
    typedef __attribute__((address_space(3))) float lds_f;
    const unsigned cnt_a = (unsigned)(size_t)(lds_f*)&lds[L_CNT + 2], rd_a = (unsigned)(size_t)(lds_f*)&lds[L_SC + 2 * lane];
    unsigned old;
    unsigned long long sv;
    f32x4 s0, s1, s2, s3;
    asm volatile(
        "s_mov_b64 %[sv], exec\n\ts_mov_b64 exec, 1\n\t"
        "ds_add_rtn_u32 %[old], %[ca], %[one]\n\t"
        "s_mov_b64 exec, %[sv]\n\t"
        "ds_read2st64_b64 %[s0], %[ra] offset1:2\n\t"
        "ds_read2st64_b64 %[s1], %[ra] offset0:4 offset1:6\n\t"
        "ds_read2st64_b64 %[s2], %[ra] offset0:8 offset1:10\n\t"
        "ds_read2st64_b64 %[s3], %[ra] offset0:12 offset1:14\n\t"
        "s_waitcnt lgkmcnt(0)"
        : [sv] "=&s"(sv), [old] "=&v"(old), [s0] "=&v"(s0), [s1] "=&v"(s1), [s2] "=&v"(s2), [s3] "=&v"(s3)
        : [ca] "v"(cnt_a), [one] "v"(1u), [ra] "v"(rd_a)
        : "memory");
    if (__builtin_amdgcn_readfirstlane(old) == NWAVE - 1) {
      const f32x4 u = (s0 + s1) + (s2 + s3);
      PUBLISH(u[0] + u[2], u[1] + u[3], o_so + mem * 1024 + lane * 16);
    }
  }
  {
    const int o_vo = ((lane >> 3) & 3) * 1024 + (8 * w + (lane & 7)) * 16;
    SB();
    WTR((8 - WL) / 2, PJ)
    _Pragma("unroll") for (int i = 0; i < 8; ++i) asm volatile("" : "+v"(wb[i]));
    SB();
    u32x4 og = BLD(wsr, o_vo, o_so, AUX_SC1);
    SB();
    WTR(PJ, 4)
    _Pragma("unroll") for (int i = 0; i < 8; ++i) asm volatile("" : "+v"(wb[i]));
    SB();
    if (!__all(TAG_OK(og))) {
#pragma unroll 1
      for (unsigned spins = 0; spins < SPIN_CAP; ++spins) {
        asm volatile("" ::: "memory");
        og = BLD(wsr, o_vo, o_so, AUX_SC1);
        if (__all(TAG_OK(og))) break;
      }
    }
    float o0 = __uint_as_float(og[0]), o1 = __uint_as_float(og[2]);
    o0 += dpp<ROR8>(o0);
    o1 += dpp<ROR8>(o1);
    reinterpret_cast<unsigned*>(lds + L_OB)[w * 16 + (g & 1) * 8 + (lane & 7)] = cvtpk(o0, o1);
  }
  WAVE_FENCE();
  STAMP(5);
  {
    const u32x4 oa = *reinterpret_cast<const u32x4*>(lds + oa_off);
    float y[8];
#pragma unroll
    for (int i = 0; i < 8; ++i)
      y[i] = __builtin_amdgcn_mfma_f32_16x16x32_bf16(__builtin_bit_cast(bf16x8, oa), __builtin_bit_cast(bf16x8, wb[i]), f32x4{0.f, 0.f, 0.f, 0.f}, 0, 0, 0)[0];
    if ((lane & 31) < 20) {
      float* yp = &lds[L_Y + w * 320 + (lane >> 5) * 160 + 8 * (lane & 31)];
      *reinterpret_cast<f32x4*>(yp) = f32x4{y[0], y[1], y[2], y[3]};
      *reinterpret_cast<f32x4*>(yp + 4) = f32x4{y[4], y[5], y[6], y[7]};
    }
  }
  __syncthreads();
  if (tid < 160) {
    f32x2 s[8];
#pragma unroll
    for (int ww = 0; ww < 8; ++ww) s[ww] = *reinterpret_cast<const f32x2*>(&lds[L_Y + ww * 320 + 2 * tid]);
    const f32x2 t = ((s[0] + s[1]) + (s[2] + s[3])) + ((s[4] + s[5]) + (s[6] + s[7]));
    PUBLISH(t[0], t[1], y_so + (h * 4 + mem) * 2560 + tid * 16);
  }
  STAMP(6);
  const int unit = wgi & 127;
  if (w == 3 && unit < 80) {
    const int y_vo = (lane >> 3) * (4 * 2560) + unit * 128 + (lane & 7) * 16;
    int out_vo = lane < 8 ? (((unit / 10) & 1) * (2 * DM) + br * DM + (unit / 20) * 160 + (unit % 10) * 16 + 2 * lane) * 2 : OOB;
    asm volatile("" : "+v"(out_vo));
    __builtin_amdgcn_s_sleep(Y_DELAY);
    u32x4 yg[4];
#pragma unroll 1
    for (unsigned spins = 0; spins < SPIN_CAP; ++spins) {
      asm volatile("" ::: "memory");
#pragma unroll
      for (int i = 0; i < 4; ++i) yg[i] = BLD(wsr, y_vo + i * (8 * 4 * 2560), y_so, AUX_SC1);
      unsigned bad = 0;
#pragma unroll
      for (int i = 0; i < 4; ++i) bad |= (yg[i][1] ^ tagA) | (yg[i][3] ^ tagB);
      if (__all(bad == 0)) break;
    }
    const float s0 = (__uint_as_float(yg[0][0]) + __uint_as_float(yg[1][0])) + (__uint_as_float(yg[2][0]) + __uint_as_float(yg[3][0]));
    const float s1 = (__uint_as_float(yg[0][2]) + __uint_as_float(yg[1][2])) + (__uint_as_float(yg[2][2]) + __uint_as_float(yg[3][2]));
    float s = fold32(s0, s1);
    s = fold16(s, s);
    s += dpp<ROR8>(s);
    auto t = __builtin_amdgcn_permlane32_swap(__float_as_uint(s), __float_as_uint(s), false, false);
    __builtin_amdgcn_raw_buffer_store_b32(cvtpk(__uint_as_float(t[0]), __uint_as_float(t[1])), RSRC(pout, NB * 2 * DM * 2), out_vo, 0, 0);
    if (unit == 0 && lane == 0) __builtin_amdgcn_raw_buffer_store_b128(u32x4{e0 + 1u, e1 + 1u, e2 + 1u, e3 + 1u}, wsr, WS_EPOCH + br * 256, 0, AUX_SC1);
  }
}

}

extern "C" void kernel_launch(void* const* d_in, const int* in_sizes, int n_in, void* d_out, int out_size, void* d_ws,
                              size_t ws_size, hipStream_t stream) {
  Args a;
  for (int br = 0; br < 2; ++br) {
    a.x[br] = (const char*)d_in[5 * br + 0];
    a.k[br] = (const char*)d_in[5 * br + 1];
    a.v[br] = (const char*)d_in[5 * br + 2];
    a.wq[br] = (const char*)d_in[5 * br + 3];
    a.wo[br] = (const char*)d_in[5 * br + 4];
  }
  a.ws = (char*)d_ws;
  a.out = (unsigned short*)d_out;
  hipLaunchKernelGGL(attn_fused, dim3(NWG), dim3(NTHR), 0, stream, a);
}
```

```cpp
#include <hip/hip_runtime.h>
#include <hip/hip_bf16.h>
#include <stdint.h>

namespace {

constexpr int DM = 640, NH = 32, SK = 4096, DH = 128, NB = 2;
constexpr int NSPLIT = 2;
constexpr int NWAVE = 8, NTHR = NWAVE * 64;
constexpr int KEYS_WG = SK / NSPLIT;
constexpr int NSTEP = KEYS_WG / (4 * NWAVE);
constexpr int STEP_B = NWAVE * 1024;
constexpr int RING = 16;
constexpr int NBODY = NSTEP / RING;
constexpr int FLIGHT = 4;
constexpr int NWG = 2 * NB * NH * NSPLIT;
constexpr int AUX_NT = 2, AUX_SC1 = 16;
constexpr unsigned SPIN_CAP = 1u << 19;
constexpr int OOB = 0x40000000;
constexpr int TAU = 6930;
constexpr int T_OFF = 140;
constexpr int DSHIFT = 6;
constexpr int LEAD = -3;
constexpr int Y_DELAY = 25;

constexpr int WS_EPOCH = 0;
constexpr int WS_Q = 4096;
constexpr int WS_ML = WS_Q + 64 * 4 * 64 * 16;
constexpr int WS_O = WS_ML + 256 * 128;
constexpr int WS_Y = WS_O + 64 * 4 * 64 * 16;
constexpr int WS_END = WS_Y + 2 * 32 * 4 * 2 * 80 * 16;

typedef unsigned u32x4 __attribute__((ext_vector_type(4)));
typedef unsigned u32x3 __attribute__((ext_vector_type(3)));
typedef float f32x4 __attribute__((ext_vector_type(4)));
typedef float f32x2 __attribute__((ext_vector_type(2)));
typedef __bf16 bf16x2 __attribute__((ext_vector_type(2)));
typedef __bf16 bf16x8 __attribute__((ext_vector_type(8)));

__device__ __forceinline__ float dot2(unsigned a, unsigned b, float acc) {
  return __builtin_amdgcn_fdot2_f32_bf16(__builtin_bit_cast(bf16x2, a), __builtin_bit_cast(bf16x2, b), acc, false);
}
__device__ __forceinline__ unsigned cvtpk(float lo, float hi) {
  bf16x2 r;
  r[0] = (__bf16)lo;
  r[1] = (__bf16)hi;
  return __builtin_bit_cast(unsigned, r);
}
template <int CTRL> __device__ __forceinline__ float dpp(float x) {
  return __builtin_bit_cast(float, __builtin_amdgcn_mov_dpp(__builtin_bit_cast(int, x), CTRL, 0xf, 0xf, true));
}
constexpr int XOR1 = 0xB1, XOR2 = 0x4E, XOR7 = 0x141, ROR8 = 0x128;
__device__ __forceinline__ unsigned perm_lo(unsigned a, unsigned b) { return __builtin_amdgcn_perm(b, a, 0x05040100u); }
__device__ __forceinline__ unsigned perm_hi(unsigned a, unsigned b) { return __builtin_amdgcn_perm(b, a, 0x07060302u); }
__device__ __forceinline__ float bf_lo(unsigned u) { return __uint_as_float(u << 16); }
__device__ __forceinline__ float bf_hi(unsigned u) { return __uint_as_float(u & 0xffff0000u); }

__device__ __forceinline__ float xrow_max(float x) {
  auto s = __builtin_amdgcn_permlane16_swap(__float_as_uint(x), __float_as_uint(x), false, false);
  x = fmaxf(__uint_as_float(s[0]), __uint_as_float(s[1]));
  auto t = __builtin_amdgcn_permlane32_swap(__float_as_uint(x), __float_as_uint(x), false, false);
  return fmaxf(__uint_as_float(t[0]), __uint_as_float(t[1]));
}
__device__ __forceinline__ float fold32(float a, float b) {
  auto t = __builtin_amdgcn_permlane32_swap(__float_as_uint(a), __float_as_uint(b), false, false);
  return __uint_as_float(t[0]) + __uint_as_float(t[1]);
}
__device__ __forceinline__ float fold16(float a, float b) {
  auto t = __builtin_amdgcn_permlane16_swap(__float_as_uint(a), __float_as_uint(b), false, false);
  return __uint_as_float(t[0]) + __uint_as_float(t[1]);
}
__device__ __forceinline__ float wave_sum(float x) {
  x += dpp<XOR1>(x); x += dpp<XOR2>(x); x += dpp<XOR7>(x); x += dpp<ROR8>(x);
  x = fold16(x, x);
  return fold32(x, x);
}
template <int N> __device__ __forceinline__ void wait_vm() {
  static_assert(N >= 0 && N < 64, "");
  __builtin_amdgcn_s_waitcnt((N & 15) | (7 << 4) | (15 << 8) | ((N >> 4) << 14));
}
template <int X> __device__ __forceinline__ void wait_lvl(int lvl) {
  asm volatile(
      "s_waitcnt vmcnt(%2+5)\n\t"
      "s_cmp_gt_i32 %0, 2\n\ts_cbranch_scc1 .Lw%=\n\ts_waitcnt vmcnt(%2+4)\n\t"
      "s_cmp_gt_i32 %0, 1\n\ts_cbranch_scc1 .Lw%=\n\ts_waitcnt vmcnt(%2+3)\n\t"
      "s_cmp_gt_i32 %0, 0\n\ts_cbranch_scc1 .Lw%=\n\ts_waitcnt vmcnt(%2+2)\n\t"
      "s_cmp_gt_i32 %0, %1\n\ts_cbranch_scc1 .Lw%=\n\ts_waitcnt vmcnt(%2+1)\n"
      ".Lw%=:"
      :: "s"(lvl), "i"(LEAD), "i"(X) : "scc", "memory");
}
__device__ __forceinline__ unsigned now_ticks() { return (unsigned)__builtin_amdgcn_s_memrealtime(); }

struct Args {
  const char* x[2];
  const char* k[2];
  const char* v[2];
  const char* wq[2];
  const char* wo[2];
  char* ws;
  unsigned short* out;
};

#define RSRC(p, n) __builtin_amdgcn_make_buffer_rsrc((void*)(p), 0, (n), 0x00020000)
#define BLD(r, vo, so, aux) __builtin_amdgcn_raw_buffer_load_b128((r), (vo), (so), (aux))
#define PUBLISH(v0, v1, off) __builtin_amdgcn_raw_buffer_store_b128(u32x4{__float_as_uint(v0), tagA, __float_as_uint(v1), tagB}, wsr, (off), 0, AUX_SC1)
#define TAG_OK(v) ((v)[1] == tagA && (v)[3] == tagB)
#define SB() __builtin_amdgcn_sched_barrier(0)
#define WAVE_FENCE() __builtin_amdgcn_fence(__ATOMIC_ACQ_REL, "wavefront")

constexpr int L_SC = 0;
constexpr int L_Y = 2048;
constexpr int L_ML = L_Y + 8 * 320;
constexpr int L_OB = L_ML + 16;
constexpr int L_CNT = L_OB + 8 * 16;
constexpr int L_ZERO = L_CNT + 4;
constexpr int L_TOT = 21 * 1024;
static_assert(L_ZERO + 4 <= L_TOT && L_ZERO % 4 == 0, "");

__device__ __forceinline__ bool last_arriver(float* lds, int i) {
  unsigned old = 0;
  if ((threadIdx.x & 63) == 0) old = __hip_atomic_fetch_add(reinterpret_cast<unsigned*>(lds + L_CNT) + i, 1u, __ATOMIC_ACQ_REL, __HIP_MEMORY_SCOPE_WORKGROUP);
  return __builtin_amdgcn_readfirstlane(old) == NWAVE - 1;
}

__global__ void __launch_bounds__(NTHR, 2) attn_fused(Args a) {
  __shared__ __attribute__((aligned(16))) float lds[L_TOT];
  const int tid = threadIdx.x, lane = tid & 63, w = __builtin_amdgcn_readfirstlane(tid >> 6);
  const int g = lane >> 4, c = lane & 15;
  const unsigned t_start = now_ticks();
  if (tid < 8) reinterpret_cast<unsigned*>(lds + L_CNT)[tid] = 0u;
  const char *px0 = a.x[0], *px1 = a.x[1], *pk0 = a.k[0], *pk1 = a.k[1], *pv0 = a.v[0], *pv1 = a.v[1], *pq0 = a.wq[0], *pq1 = a.wq[1], *po0 = a.wo[0], *po1 = a.wo[1];
  char* pws = a.ws;
  unsigned short* pout = a.out;
  asm volatile("" : "+s"(px0), "+s"(px1), "+s"(pk0), "+s"(pk1), "+s"(pv0), "+s"(pv1), "+s"(pq0), "+s"(pq1), "+s"(po0), "+s"(po1), "+s"(pws), "+s"(pout));
  const int bid = blockIdx.x, xcd = bid & 7, idx = bid >> 3;
  const int grp = xcd * 8 + (idx >> 2), mem = idx & 3;
  const int br = grp >> 5, h = grp & 31, b = mem >> 1, sp = mem & 1;
  const int wgi = grp * 4 + mem;
  const int rot = wgi & (NSTEP - 1);
  const auto wsr = RSRC(pws, WS_END);
  int q_so = WS_Q + grp * 4096, ml_so = WS_ML + (wgi & ~1) * 128, o_so = WS_O + grp * 4096, y_so = WS_Y + br * (32 * 4 * 2 * 80 * 16);
  asm volatile("" : "+s"(q_so), "+s"(ml_so), "+s"(o_so), "+s"(y_so));

  const u32x4 epoch_v = BLD(wsr, 0, WS_EPOCH + br * 256, AUX_SC1 | AUX_NT);
  const int row0 = 320 * b + 40 * w + 5 * (lane >> 3);
  u32x3 xr[2];
  {
    const auto xrs = RSRC(br ? px1 : px0, NB * DM * 2);
#pragma unroll
    for (int bb = 0; bb < 2; ++bb) xr[bb] = __builtin_amdgcn_raw_buffer_load_b96(xrs, (row0 * 2) & ~3, bb * DM * 2, AUX_NT);
  }
  const auto wqr = RSRC(br ? pq1 : pq0, DM * NH * DH * 2);
  u32x4 wq[5];
#pragma unroll
  for (int j = 0; j < 5; ++j) wq[j] = BLD(wqr, row0 * (NH * DH * 2) + 16 * (lane & 7), (h * DH + sp * 64) * 2 + j * (NH * DH * 2), AUX_NT);
  SB();
  __syncthreads();
  SB();
  const size_t kvoff = ((size_t)(b * NH + h) * SK + sp * KEYS_WG) * (DH * 2);
  const auto kr = RSRC((br ? pk1 : pk0) + kvoff, KEYS_WG * DH * 2);
  const auto vr = RSRC((br ? pv1 : pv0) + kvoff, KEYS_WG * DH * 2);
  const int kv_vo = w * 1024 + lane * 16;
#define SOFF(s) ((((s) + rot) & (NSTEP - 1)) * STEP_B)
  u32x4 ring[RING];
#pragma unroll
  for (int t = 0; t < FLIGHT; ++t) ring[t] = BLD(kr, kv_vo, SOFF(t), AUX_NT);
  SB();
  const unsigned e0 = __builtin_amdgcn_readfirstlane(epoch_v[0]), e1 = __builtin_amdgcn_readfirstlane(epoch_v[1]),
                 e2 = __builtin_amdgcn_readfirstlane(epoch_v[2]), e3 = __builtin_amdgcn_readfirstlane(epoch_v[3]);
  const unsigned epoch = e0 + (e1 - e0) * 0x10001u + (e2 - e0) * 0x101u + (e3 - e0) * 0x1001u;
  const unsigned tagA = epoch + 1u, tagB = epoch ^ 0x9E3779B9u;

  {
    f32x2 qa[2][4];
#pragma unroll
    for (int bb = 0; bb < 2; ++bb) {
      const unsigned sh = (row0 & 1) * 16;
      const unsigned x01 = __builtin_amdgcn_alignbit(xr[bb][1], xr[bb][0], sh), x23 = __builtin_amdgcn_alignbit(xr[bb][2], xr[bb][1], sh), x4 = xr[bb][2] >> sh;
      const float xv[5] = {bf_lo(x01), bf_hi(x01), bf_lo(x23), bf_hi(x23), bf_lo(x4)};
#pragma unroll
      for (int d = 0; d < 4; ++d) {
        qa[bb][d] = f32x2{0.f, 0.f};
#pragma unroll
        for (int j = 0; j < 5; ++j) qa[bb][d] = __builtin_elementwise_fma(f32x2{bf_lo(wq[j][d]), bf_hi(wq[j][d])}, f32x2{xv[j], xv[j]}, qa[bb][d]);
      }
    }
    float f[8], e[4];
#pragma unroll
    for (int i = 0; i < 8; ++i) f[i] = fold32(qa[0][i >> 1][i & 1], qa[1][i >> 1][i & 1]);
#pragma unroll
    for (int i = 0; i < 4; ++i) { e[i] = fold16(f[i], f[i + 4]); e[i] += dpp<ROR8>(e[i]); }
    if (c < 8) *reinterpret_cast<f32x4*>(&lds[L_Y + w * 128 + (g >> 1) * 64 + 8 * c + 4 * (g & 1)]) = f32x4{e[0], e[1], e[2], e[3]};
  }
  if (last_arriver(lds, 0)) {
    f32x2 s = {0.f, 0.f};
#pragma unroll
    for (int ww = 0; ww < NWAVE; ++ww) s += *reinterpret_cast<const f32x2*>(&lds[L_Y + ww * 128 + 2 * lane]);
    PUBLISH(s[0], s[1], q_so + (((lane >> 5) * 2 + b) * 64 + sp * 32 + (lane & 31)) * 16);
  }
  const auto wor = RSRC((br ? po1 : po0) + (size_t)h * DH * DM * 2, DH * DM * 2);
  const int wo_vo = (lane & 31) < 20 ? (16 * w + 8 * (lane >> 5)) * (DM * 2) + mem * 320 + 16 * (lane & 31) : OOB;
  u32x4 wo[8];
  u32x4 qg0, qg1;
  const int q_vo = (b * 128 + lane) * 16;
#pragma unroll
  for (int t = FLIGHT; t < RING; ++t) {
    wait_vm<FLIGHT - 1>();
    ring[t] = BLD(kr, kv_vo, SOFF(t), AUX_NT);
    SB();
  }
  qg0 = BLD(wsr, q_vo, q_so, AUX_SC1); qg1 = BLD(wsr, q_vo + 1024, q_so, AUX_SC1);
#pragma unroll
  for (int j = 0; j < 4; ++j) {
    wait_vm<FLIGHT + 1>();
    wo[j] = BLD(wor, wo_vo, j * (DM * 2), AUX_NT);
    SB();
  }
  if (!__all(TAG_OK(qg0) && TAG_OK(qg1))) {
#pragma unroll 1
    for (unsigned spins = 0; spins < SPIN_CAP; ++spins) {
      asm volatile("" ::: "memory");
      qg0 = BLD(wsr, q_vo, q_so, AUX_SC1); qg1 = BLD(wsr, q_vo + 1024, q_so, AUX_SC1);
      if (__all(TAG_OK(qg0) && TAG_OK(qg1))) break;
      __builtin_amdgcn_s_sleep(1);
    }
  }
  reinterpret_cast<unsigned*>(lds + L_SC)[w * 256 + lane] = cvtpk(__uint_as_float(qg0[0]) + __uint_as_float(qg1[0]), __uint_as_float(qg0[2]) + __uint_as_float(qg1[2]));
  WAVE_FENCE();
  const u32x4 qv = *reinterpret_cast<const u32x4*>(lds + L_SC + w * 256 + 4 * c);
  WAVE_FENCE();
  __builtin_amdgcn_s_barrier();

  unsigned t_ref = t_start + T_OFF + (((2 * (5 + RING) + 5) * TAU) >> 9);
  int lvl;
#define RELAG(T) { lvl = (int)(now_ticks() - t_ref - (unsigned)(((T) * TAU) >> 8)) >> DSHIFT; asm volatile("" : "+s"(lvl)); SB(); }

  float* scw = &lds[L_SC + (w * 4 + g) * NSTEP];
  float mx = -INFINITY;
#define KSTEP(T)                                                                        \
  {                                                                                     \
    float d = dot2(ring[T][0], qv[0], 0.f);                                             \
    d = dot2(ring[T][1], qv[1], d);                                                     \
    d = dot2(ring[T][2], qv[2], d);                                                     \
    d = dot2(ring[T][3], qv[3], d);                                                     \
    x[(T) & 3] = d;                                                                     \
    asm volatile("" : "+v"(x[(T) & 3]));                                                \
    SB();                                                                               \
    wait_lvl<0>(lvl);                                                                   \
    ring[T] = BLD(nr, kv_vo, SOFF(RING * it + RING + (T)), AUX_NT);                     \
    SB();                                                                               \
    if (((T) & 3) == 3) {     \
      const float a0 = x[0] + dpp<ROR8>(x[0]), a1 = x[1] + dpp<ROR8>(x[1]), a2 = x[2] + dpp<ROR8>(x[2]), a3 = x[3] + dpp<ROR8>(x[3]); \
      const float b0 = (c & 8) ? a2 : a0, b1 = (c & 8) ? a3 : a1;                       \
      const float c0 = b0 + dpp<XOR7>(b0), c1 = b1 + dpp<XOR7>(b1);                     \
      float s = (c & 4) ? c1 : c0;                                                      \
      s += dpp<XOR2>(s);                                                                \
      s += dpp<XOR1>(s);                                                                \
      mx = fmaxf(mx, s);                                                                \
      scw[RING * it + (T) - 3 + (c >> 2)] = s;                                          \
      SB();                                                                             \
      if ((T) != 15) RELAG((T) + 1)                                                     \
    }                                                                                   \
  }
#pragma unroll 1
  for (int it = 0; it < NBODY; ++it) {
    const auto nr = (it == NBODY - 1) ? vr : kr;
    float x[4];
    RELAG(0)
    KSTEP(0) KSTEP(1) KSTEP(2) KSTEP(3) KSTEP(4) KSTEP(5) KSTEP(6) KSTEP(7)
    KSTEP(8) KSTEP(9) KSTEP(10) KSTEP(11) KSTEP(12) KSTEP(13) KSTEP(14) KSTEP(15)
    t_ref += (RING * TAU) >> 8;
  }
#undef KSTEP
  constexpr float C2 = 0.088388347648318440f * 1.4426950408889634f;
  mx = fmaxf(mx, dpp<ROR8>(mx));
  mx = fmaxf(mx, dpp<XOR7>(mx));
  mx = xrow_max(mx) * C2;
  WAVE_FENCE();
  {
    f32x4 s4 = *reinterpret_cast<const f32x4*>(scw + 4 * c);
#pragma unroll
    for (int t = 0; t < 4; ++t) s4[t] = __builtin_amdgcn_exp2f(fmaf(s4[t], C2, -mx));
    *reinterpret_cast<f32x4*>(scw + 4 * c) = s4;
    const float ls = wave_sum((s4[0] + s4[1]) + (s4[2] + s4[3]));
    if (lane == 0) *reinterpret_cast<f32x2*>(&lds[L_ML + 2 * w]) = f32x2{mx, ls};
  }
  if (last_arriver(lds, 1)) {
    f32x4 ml[4];
#pragma unroll
    for (int i = 0; i < 4; ++i) ml[i] = *reinterpret_cast<const f32x4*>(&lds[L_ML + 4 * i]);
    const float mwg = fmaxf(fmaxf(fmaxf(ml[0][0], ml[0][2]), fmaxf(ml[1][0], ml[1][2])), fmaxf(fmaxf(ml[2][0], ml[2][2]), fmaxf(ml[3][0], ml[3][2])));
    float lwg = 0.f;
#pragma unroll
    for (int i = 0; i < 4; ++i) lwg += ml[i][1] * __builtin_amdgcn_exp2f(ml[i][0] - mwg) + ml[i][3] * __builtin_amdgcn_exp2f(ml[i][2] - mwg);
    if (lane == 0) PUBLISH(mwg, lwg, ml_so + sp * 128);
  }
  u32x4 wb[8];
#define WTR(J0, J1) { _Pragma("unroll") for (int i = 0; i < 8; ++i) { _Pragma("unroll") for (int jj = (J0); jj < (J1); ++jj)                      \
      wb[i][jj] = (i & 1) ? perm_hi(wo[2 * jj][i >> 1], wo[2 * jj + 1][i >> 1]) : perm_lo(wo[2 * jj][i >> 1], wo[2 * jj + 1][i >> 1]); }  \
    _Pragma("unroll") for (int i = 0; i < 8; ++i) asm volatile("" : "+v"(wb[i]));                                                       \
    SB(); }
#pragma unroll
  for (int i = 0; i < 8; ++i) wb[i] = u32x4{0u, 0u, 0u, 0u};
  WTR(0, 2)

  f32x2 acc2[4];
#pragma unroll
  for (int i = 0; i < 4; ++i) acc2[i] = f32x2{0.f, 0.f};
  u32x4 mlp;
  const int ml_vo = (lane & 1) * 128;
#define VSTEP(T, LOAD)                                                                  \
  {                                                                                     \
    if (((T) & 3) == 0) p4 = *reinterpret_cast<const f32x4*>(scw + RING * it + (T));    \
    _Pragma("unroll") for (int d = 0; d < 4; ++d)                                       \
      acc2[d] = __builtin_elementwise_fma(f32x2{bf_lo(ring[T][d]), bf_hi(ring[T][d])}, f32x2{p4[(T) & 3], p4[(T) & 3]}, acc2[d]); \
    asm volatile("" : "+v"(acc2[0]), "+v"(acc2[1]), "+v"(acc2[2]), "+v"(acc2[3]));     \
    SB();                                                                               \
    if (LOAD) { wait_lvl<((T) < 3) ? 1 : 0>(lvl); ring[T] = BLD(vr, kv_vo, SOFF(RING * it + RING + (T)), AUX_NT); } \
    SB();                                                                               \
    if (LOAD && ((T) & 3) == 3 && (T) != 15) RELAG((T) + 1)                             \
    if (!(LOAD) && (T) < 4) { wo[(4 + (T)) & 7] = BLD(wor, wo_vo, (4 + (T)) * (DM * 2), AUX_NT); SB(); } \
  }
#define VBODY(LOAD) { f32x4 p4;                                                         \
    VSTEP(0, LOAD) VSTEP(1, LOAD) VSTEP(2, LOAD) VSTEP(3, LOAD) VSTEP(4, LOAD) VSTEP(5, LOAD) VSTEP(6, LOAD) VSTEP(7, LOAD) \
    VSTEP(8, LOAD) VSTEP(9, LOAD) VSTEP(10, LOAD) VSTEP(11, LOAD) VSTEP(12, LOAD) VSTEP(13, LOAD) VSTEP(14, LOAD) VSTEP(15, LOAD) }
#pragma unroll 1
  for (int it = 0; it < NBODY - 1; ++it) {
    mlp = BLD(wsr, it == NBODY - 2 ? ml_vo : OOB, ml_so, AUX_SC1);
    SB();
    RELAG(0)
    VBODY(true)
    t_ref += (RING * TAU) >> 8;
  }
  if (!__all(TAG_OK(mlp))) {
#pragma unroll 1
    for (unsigned spins = 0; spins < SPIN_CAP; ++spins) {
      asm volatile("" ::: "memory");
      mlp = BLD(wsr, ml_vo, ml_so, AUX_SC1);
      if (__all(TAG_OK(mlp))) break;
      __builtin_amdgcn_s_sleep(1);
    }
  }
  float scale;
  {
    const float m1 = __uint_as_float(mlp[0]), l1 = __uint_as_float(mlp[2]);
    const float M = fmaxf(m1, dpp<XOR1>(m1));
    const float t = l1 * __builtin_amdgcn_exp2f(m1 - M);
    scale = __builtin_amdgcn_exp2f(mx - M) / (t + dpp<XOR1>(t));
  }
  int oa_off = ((g & 1) == ((c >> 2) & 1)) ? L_OB + w * 16 + (c >> 3) * 8 + 4 * (g >> 1) : L_ZERO;
  asm volatile("" : "+v"(scale), "+v"(oa_off));
  SB();
  { const int it = NBODY - 1; VBODY(false) }
#undef VBODY
#undef VSTEP
  WAVE_FENCE();
  {
    const float f0 = fold32(acc2[0][0], acc2[2][0]), f1 = fold32(acc2[0][1], acc2[2][1]), f2 = fold32(acc2[1][0], acc2[3][0]), f3 = fold32(acc2[1][1], acc2[3][1]);
    const float r0 = fold16(f0, f2), r1 = fold16(f1, f3);
    *reinterpret_cast<f32x2*>(&lds[L_SC + w * 256 + 8 * c + 2 * (g & 1) + 4 * (g >> 1)]) = f32x2{r0 * scale, r1 * scale};
    typedef __attribute__((address_space(3))) float lds_f;
    const unsigned cnt_a = (unsigned)(size_t)(lds_f*)&lds[L_CNT + 2], rd_a = (unsigned)(size_t)(lds_f*)&lds[L_SC + 2 * lane];
    unsigned old;
    unsigned long long sv;
    f32x4 s0, s1, s2, s3;
    asm volatile(
        "s_mov_b64 %[sv], exec\n\ts_mov_b64 exec, 1\n\t"
        "ds_add_rtn_u32 %[old], %[ca], %[one]\n\t"
        "s_mov_b64 exec, %[sv]\n\t"
        "ds_read2st64_b64 %[s0], %[ra] offset1:2\n\t"
        "ds_read2st64_b64 %[s1], %[ra] offset0:4 offset1:6\n\t"
        "ds_read2st64_b64 %[s2], %[ra] offset0:8 offset1:10\n\t"
        "ds_read2st64_b64 %[s3], %[ra] offset0:12 offset1:14\n\t"
        "s_waitcnt lgkmcnt(0)"
        : [sv] "=&s"(sv), [old] "=&v"(old), [s0] "=&v"(s0), [s1] "=&v"(s1), [s2] "=&v"(s2), [s3] "=&v"(s3)
        : [ca] "v"(cnt_a), [one] "v"(1u), [ra] "v"(rd_a)
        : "memory");
    if (__builtin_amdgcn_readfirstlane(old) == NWAVE - 1) {
      const f32x4 u = (s0 + s1) + (s2 + s3);
      PUBLISH(u[0] + u[2], u[1] + u[3], o_so + mem * 1024 + lane * 16);
    }
  }
  {
    const int o_vo = ((lane >> 3) & 3) * 1024 + (8 * w + (lane & 7)) * 16;
    SB();
    WTR(2, 3)
    u32x4 og = BLD(wsr, o_vo, o_so, AUX_SC1);
    SB();
    WTR(3, 4)
    if (!__all(TAG_OK(og))) {
#pragma unroll 1
      for (unsigned spins = 0; spins < SPIN_CAP; ++spins) {
        asm volatile("" ::: "memory");
        og = BLD(wsr, o_vo, o_so, AUX_SC1);
        if (__all(TAG_OK(og))) break;
      }
    }
    float o0 = __uint_as_float(og[0]), o1 = __uint_as_float(og[2]);
    o0 += dpp<ROR8>(o0);
    o1 += dpp<ROR8>(o1);
    reinterpret_cast<unsigned*>(lds + L_OB)[w * 16 + (g & 1) * 8 + (lane & 7)] = cvtpk(o0, o1);
  }
  WAVE_FENCE();
  {
    const u32x4 oa = *reinterpret_cast<const u32x4*>(lds + oa_off);
    float y[8];
#pragma unroll
    for (int i = 0; i < 8; ++i)
      y[i] = __builtin_amdgcn_mfma_f32_16x16x32_bf16(__builtin_bit_cast(bf16x8, oa), __builtin_bit_cast(bf16x8, wb[i]), f32x4{0.f, 0.f, 0.f, 0.f}, 0, 0, 0)[0];
    if ((lane & 31) < 20) {
      float* yp = &lds[L_Y + w * 320 + (lane >> 5) * 160 + 8 * (lane & 31)];
      *reinterpret_cast<f32x4*>(yp) = f32x4{y[0], y[1], y[2], y[3]};
      *reinterpret_cast<f32x4*>(yp + 4) = f32x4{y[4], y[5], y[6], y[7]};
    }
  }
  __syncthreads();
  if (tid < 160) {
    f32x2 s[8];
#pragma unroll
    for (int ww = 0; ww < 8; ++ww) s[ww] = *reinterpret_cast<const f32x2*>(&lds[L_Y + ww * 320 + 2 * tid]);
    const f32x2 t = ((s[0] + s[1]) + (s[2] + s[3])) + ((s[4] + s[5]) + (s[6] + s[7]));
    PUBLISH(t[0], t[1], y_so + (h * 4 + mem) * 2560 + tid * 16);
  }
  const int unit = wgi & 127;
  if (w == 3 && unit < 80) {
    const int y_vo = (lane >> 3) * (4 * 2560) + unit * 128 + (lane & 7) * 16;
    int out_vo = lane < 8 ? (((unit / 10) & 1) * (2 * DM) + br * DM + (unit / 20) * 160 + (unit % 10) * 16 + 2 * lane) * 2 : OOB;
    asm volatile("" : "+v"(out_vo));
    __builtin_amdgcn_s_sleep(Y_DELAY);
    u32x4 yg[4];
#pragma unroll 1
    for (unsigned spins = 0; spins < SPIN_CAP; ++spins) {
      asm volatile("" ::: "memory");
#pragma unroll
      for (int i = 0; i < 4; ++i) yg[i] = BLD(wsr, y_vo + i * (8 * 4 * 2560), y_so, AUX_SC1);
      unsigned bad = 0;
#pragma unroll
      for (int i = 0; i < 4; ++i) bad |= (yg[i][1] ^ tagA) | (yg[i][3] ^ tagB);
      if (__all(bad == 0)) break;
    }
    const float s0 = (__uint_as_float(yg[0][0]) + __uint_as_float(yg[1][0])) + (__uint_as_float(yg[2][0]) + __uint_as_float(yg[3][0]));
    const float s1 = (__uint_as_float(yg[0][2]) + __uint_as_float(yg[1][2])) + (__uint_as_float(yg[2][2]) + __uint_as_float(yg[3][2]));
    float s = fold32(s0, s1);
    s = fold16(s, s);
    s += dpp<ROR8>(s);
    auto t = __builtin_amdgcn_permlane32_swap(__float_as_uint(s), __float_as_uint(s), false, false);
    __builtin_amdgcn_raw_buffer_store_b32(cvtpk(__uint_as_float(t[0]), __uint_as_float(t[1])), RSRC(pout, NB * 2 * DM * 2), out_vo, 0, 0);
    if (unit == 0 && lane == 0) __builtin_amdgcn_raw_buffer_store_b128(u32x4{e0 + 1u, e1 + 1u, e2 + 1u, e3 + 1u}, wsr, WS_EPOCH + br * 256, 0, AUX_SC1);
  }
}

}

extern "C" void kernel_launch(void* const* d_in, const int* in_sizes, int n_in, void* d_out, int out_size, void* d_ws,
                              size_t ws_size, hipStream_t stream) {
  Args a;
  for (int br = 0; br < 2; ++br) {
    a.x[br] = (const char*)d_in[5 * br + 0];
    a.k[br] = (const char*)d_in[5 * br + 1];
    a.v[br] = (const char*)d_in[5 * br + 2];
    a.wq[br] = (const char*)d_in[5 * br + 3];
    a.wo[br] = (const char*)d_in[5 * br + 4];
  }
  a.ws = (char*)d_ws;
  a.out = (unsigned short*)d_out;
  hipLaunchKernelGGL(attn_fused, dim3(NWG), dim3(NTHR), 0, stream, a);
}
```
